```python
import torch

def _softmax(x, axis=-1):
    return torch.softmax(x, dim=axis)
def _gelu(x):
    return torch.nn.functional.gelu(x, approximate="tanh")
def _silu(x):
    return torch.nn.functional.silu(x)
def _sigmoid(x):
    return torch.sigmoid(x)
def _layernorm(x, axis=-1, eps=1e-5):
    m = x.mean(dim=axis, keepdim=True)
    v = x.var(dim=axis, keepdim=True, unbiased=False)
    return (x - m) / torch.sqrt(v + eps)
def _scatter_add(tgt, idx, src, axis=0):
    return tgt.clone().index_add_(axis, idx, src)
def _segment_sum(src, seg, n):
    out = torch.zeros((n,) + src.shape[1:], dtype=src.dtype)
    return out.index_add_(0, seg, src)
def _pad(x, pw):
    flat = tuple(v for pair in reversed(pw) for v in pair)
    return torch.nn.functional.pad(x, flat)

kernel_name = 'sequential_preLN__compressed_kv_cla__glu_gated_reglu__decode'

def setup_inputs():

    _w = (lambda a: (lambda t: t.to(torch.bfloat16) if t.is_floating_point() else t)(torch.as_tensor(a)))
    def _A_setup():
        B, H, Sq, Sk, D, M = 4, 32, 1, 4096, 128, 512
        Dc = 4 * D
        return {
            "x": _w((lambda *s: torch.randn(*s, dtype=torch.bfloat16) * 0.05)(B, Sq, M)),
            "c_kv": _w((lambda *s: torch.randn(*s, dtype=torch.bfloat16) * 0.8)(B, Sk, Dc)),
            "w_q": _w((lambda *s: torch.randn(*s, dtype=torch.bfloat16) * 1.6)(M, H, D) * (M ** -0.5)),
            "w_uk": _w((lambda *s: torch.randn(*s, dtype=torch.bfloat16) * 1.6)(Dc, H, D) * (Dc ** -0.5)),
            "w_uv": _w((lambda *s: torch.randn(*s, dtype=torch.bfloat16) * 0.8)(Dc, H, D) * (Dc ** -0.5)),
            "w_o": _w((lambda *s: torch.randn(*s, dtype=torch.bfloat16) * 0.8)(H, D, M) * ((H * D) ** -0.5)),
        }
    _A_in = _A_setup()

    def _B_setup():
        B, S, D, F = 4, 1, 512, 1792
        return {
            "x": _w((lambda *s: torch.randn(*s, dtype=torch.bfloat16) * 0.05)(B, S, D)),
            "W_gate": _w((lambda *s: torch.randn(*s, dtype=torch.bfloat16) * 0.4)(D, F) * (2.0 / D) ** 0.5),
            "W_up": _w((lambda *s: torch.randn(*s, dtype=torch.bfloat16) * 0.4)(D, F) * (2.0 / D) ** 0.5),
            "W_down": _w((lambda *s: torch.randn(*s, dtype=torch.bfloat16) * 0.4)(F, D) * (1.0 / F) ** 0.5),
        }
    _B_in = _B_setup()

    return {
        "A_x": _A_in["x"],
        "A_c_kv": _A_in["c_kv"],
        "A_w_q": _A_in["w_q"],
        "A_w_uk": _A_in["w_uk"],
        "A_w_uv": _A_in["w_uv"],
        "A_w_o": _A_in["w_o"],
        "B_x": _B_in["x"],
        "B_W_gate": _B_in["W_gate"],
        "B_W_up": _B_in["W_up"],
        "B_W_down": _B_in["W_down"],
    }

def reference(A_x, A_c_kv, A_w_q, A_w_uk, A_w_uv, A_w_o, B_x, B_W_gate, B_W_up, B_W_down):
    def _A_ref(x, c_kv, w_q, w_uk, w_uv, w_o):
        B, Sq, M = x.shape[0], x.shape[1], x.shape[2]
        Sk = c_kv.shape[1]
        H, D = w_q.shape[1], w_q.shape[2]
        q = torch.einsum("bqm,mhd->bhqd", x, w_q)
        k = torch.einsum("bkc,chd->bhkd", c_kv, w_uk)
        v = torch.einsum("bkc,chd->bhkd", c_kv, w_uv)
        scores = torch.einsum("bhqd,bhkd->bhqk", q, k) * (D ** -0.5)
        off = Sk - Sq
        qi = torch.arange(Sq)[:, None]
        kj = torch.arange(Sk)[None, :]
        cmask = kj <= (qi + off)
        scores = torch.where(cmask[None, None], scores, -1e9)
        p = _softmax(scores, axis=-1)
        ctx = torch.einsum("bhqk,bhkd->bhqd", p, v)
        return torch.einsum("bhqd,hdm->bqm", ctx, w_o)

    def _B_ref(x, W_gate, W_up, W_down):
        gate = torch.einsum("bsd,df->bsf", x, W_gate)
        up = torch.einsum("bsd,df->bsf", x, W_up)
        gate = gate * (lambda x, dt: x.to(dt))(gate > 0, gate.dtype)
        h = gate * up
        return torch.einsum("bsf,fd->bsd", h, W_down)

    x = A_x
    h = x + _A_ref(_layernorm(x, axis=-1), A_c_kv, A_w_q, A_w_uk, A_w_uv, A_w_o)
    h2 = h + _B_ref(_layernorm(h, axis=-1), B_W_gate, B_W_up, B_W_down)
    return h2
```

```cpp
#include <hip/hip_runtime.h>
#include <hip/hip_bf16.h>
#include <stdint.h>

typedef unsigned short u16;
__device__ __forceinline__ float bf2f(u16 v) { return __uint_as_float(((unsigned)v) << 16); }
__device__ __forceinline__ u16 f2bf(float f) { unsigned u = __float_as_uint(f); u += 0x7fffu + ((u >> 16) & 1u); return (u16)(u >> 16); }

constexpr int B = 4, H = 32, SK = 4096, D = 128, M = 512, DC = 512, F = 1792;

__global__ void __launch_bounds__(512) k1(const u16* x, const u16* wq, const u16* wuk, float* qabs) {
    __shared__ float xn[B][M];
    __shared__ float q[B][D];
    int h = blockIdx.x, t = threadIdx.x;
    {
        int w = t >> 6, l = t & 63;
        if (w < B) {
            float s = 0.f, v[8];
            for (int i = 0; i < 8; ++i) { v[i] = bf2f(x[w * M + l * 8 + i]); s += v[i]; }
            for (int o = 32; o; o >>= 1) s += __shfl_xor(s, o);
            float mean = s / M, ss = 0.f;
            for (int i = 0; i < 8; ++i) { float d = v[i] - mean; ss += d * d; }
            for (int o = 32; o; o >>= 1) ss += __shfl_xor(ss, o);
            float r = rsqrtf(ss / M + 1e-5f);
            for (int i = 0; i < 8; ++i) xn[w][l * 8 + i] = (v[i] - mean) * r;
        }
    }
    __syncthreads();
    {
        int b = t >> 7, d = t & 127;
        float s = 0.f;
        for (int m = 0; m < M; ++m) s += xn[b][m] * bf2f(wq[(size_t)m * H * D + h * D + d]);
        q[b][d] = s;
    }
    __syncthreads();
    {
        int c = t;
        float s[B] = {0, 0, 0, 0};
        for (int d = 0; d < D; ++d) { float w = bf2f(wuk[(size_t)c * H * D + h * D + d]); for (int b = 0; b < B; ++b) s[b] += q[b][d] * w; }
        for (int b = 0; b < B; ++b) qabs[(b * H + h) * DC + c] = s[b] * 0.08838834764831845f;
    }
}
__global__ void __launch_bounds__(512) k2(const u16* ckv, const float* qabs, float* part, float* ml) {
    __shared__ float s[H][64];
    int b = blockIdx.x >> 6, sp = blockIdx.x & 63, t = threadIdx.x;
    const u16* kv = ckv + ((size_t)b * SK + sp * 64) * DC;
    for (int i = t; i < H * 64; i += 512) {
        int h = i >> 6, k = i & 63;
        float a = 0.f;
        for (int c = 0; c < DC; ++c) a += qabs[(b * H + h) * DC + c] * bf2f(kv[k * DC + c]);
        s[h][k] = a;
    }
    __syncthreads();
    if (t < H) {
        float m = -1e30f;
        for (int k = 0; k < 64; ++k) m = fmaxf(m, s[t][k]);
        float l = 0.f;
        for (int k = 0; k < 64; ++k) { float p = __expf(s[t][k] - m); s[t][k] = p; l += p; }
        ml[((b * H + t) * 64 + sp) * 2] = m; ml[((b * H + t) * 64 + sp) * 2 + 1] = l;
    }
    __syncthreads();
    for (int h = 0; h < H; ++h) {
        float a = 0.f;
        for (int k = 0; k < 64; ++k) a += s[h][k] * bf2f(kv[k * DC + t]);
        part[((size_t)(b * H + h) * 64 + sp) * DC + t] = a;
    }
}
__global__ void __launch_bounds__(512) k3(const float* part, const float* ml, const u16* wuv, float* ctx) {
    __shared__ float cl[DC];
    __shared__ float red[4][D];
    int bh = blockIdx.x, h = bh & 31, t = threadIdx.x;
    float m = -1e30f;
    for (int s = 0; s < 64; ++s) m = fmaxf(m, ml[(bh * 64 + s) * 2]);
    float l = 0.f, a = 0.f;
    for (int s = 0; s < 64; ++s) { float w = __expf(ml[(bh * 64 + s) * 2] - m); l += w * ml[(bh * 64 + s) * 2 + 1]; a += w * part[((size_t)bh * 64 + s) * DC + t]; }
    cl[t] = a / l;
    __syncthreads();
    int d = t & 127, g = t >> 7;
    float s = 0.f;
    for (int c = g * 128; c < g * 128 + 128; ++c) s += cl[c] * bf2f(wuv[(size_t)c * H * D + h * D + d]);
    red[g][d] = s;
    __syncthreads();
    if (t < D) ctx[bh * D + t] = red[0][t] + red[1][t] + red[2][t] + red[3][t];
}
__global__ void __launch_bounds__(512) k4(const u16* x, const float* ctx, const u16* wo, float* hbuf) {
    __shared__ float red[8][64];
    int b = blockIdx.x >> 3, n = (blockIdx.x & 7) * 64 + (threadIdx.x & 63), g = threadIdx.x >> 6;
    float s = 0.f;
    for (int k = g * 512; k < g * 512 + 512; ++k) s += ctx[b * H * D + k] * bf2f(wo[(size_t)k * M + n]);
    red[g][threadIdx.x & 63] = s;
    __syncthreads();
    if (g == 0) { float a = 0.f; for (int i = 0; i < 8; ++i) a += red[i][threadIdx.x]; hbuf[b * M + n] = bf2f(x[b * M + n]) + a; }
}
__global__ void __launch_bounds__(512) k5(const float* hbuf, const u16* wg, const u16* wu, float* act) {
    __shared__ float hn[M];
    __shared__ float rg[8][64], ru[8][64];
    int b = blockIdx.x / 28, f = (blockIdx.x % 28) * 64 + (threadIdx.x & 63), g = threadIdx.x >> 6, t = threadIdx.x;
    if (t < 64) {
        float s = 0.f, v[8];
        for (int i = 0; i < 8; ++i) { v[i] = hbuf[b * M + t * 8 + i]; s += v[i]; }
        for (int o = 32; o; o >>= 1) s += __shfl_xor(s, o);
        float mean = s / M, ss = 0.f;
        for (int i = 0; i < 8; ++i) { float d = v[i] - mean; ss += d * d; }
        for (int o = 32; o; o >>= 1) ss += __shfl_xor(ss, o);
        float r = rsqrtf(ss / M + 1e-5f);
        for (int i = 0; i < 8; ++i) hn[t * 8 + i] = (v[i] - mean) * r;
    }
    __syncthreads();
    float sg = 0.f, su = 0.f;
    for (int k = g * 64; k < g * 64 + 64; ++k) { sg += hn[k] * bf2f(wg[(size_t)k * F + f]); su += hn[k] * bf2f(wu[(size_t)k * F + f]); }
    rg[g][t & 63] = sg; ru[g][t & 63] = su;
    __syncthreads();
    if (g == 0) { float a = 0.f, c = 0.f; for (int i = 0; i < 8; ++i) { a += rg[i][t]; c += ru[i][t]; } act[b * F + f] = fmaxf(a, 0.f) * c; }
}
__global__ void __launch_bounds__(512) k6(const float* hbuf, const float* act, const u16* wd, u16* out) {
    __shared__ float red[8][64];
    int b = blockIdx.x >> 3, n = (blockIdx.x & 7) * 64 + (threadIdx.x & 63), g = threadIdx.x >> 6;
    float s = 0.f;
    for (int k = g * 224; k < g * 224 + 224; ++k) s += act[b * F + k] * bf2f(wd[(size_t)k * M + n]);
    red[g][threadIdx.x & 63] = s;
    __syncthreads();
    if (g == 0) { float a = 0.f; for (int i = 0; i < 8; ++i) a += red[i][threadIdx.x]; out[b * M + n] = f2bf(hbuf[b * M + n] + a); }
}

extern "C" void kernel_launch(void* const* d_in, const int* in_sizes, int n_in,
                              void* d_out, int out_size, void* d_ws, size_t ws_size,
                              hipStream_t stream) {
    const u16* x = (const u16*)d_in[0];
    const u16* ckv = (const u16*)d_in[1];
    const u16* wq = (const u16*)d_in[2];
    const u16* wuk = (const u16*)d_in[3];
    const u16* wuv = (const u16*)d_in[4];
    const u16* wo = (const u16*)d_in[5];
    const u16* wg = (const u16*)d_in[7];
    const u16* wu = (const u16*)d_in[8];
    const u16* wd = (const u16*)d_in[9];
    float* ws = (float*)d_ws;
    float* qabs = ws;
    float* ml = qabs + B * H * DC;
    float* ctx = ml + B * H * 64 * 2;
    float* hbuf = ctx + B * H * D;
    float* act = hbuf + B * M;
    float* part = act + B * F + 1024;
    k1<<<32, 512, 0, stream>>>(x, wq, wuk, qabs);
    k2<<<256, 512, 0, stream>>>(ckv, qabs, part, ml);
    k3<<<128, 512, 0, stream>>>(part, ml, wuv, ctx);
    k4<<<32, 512, 0, stream>>>(x, ctx, wo, hbuf);
    k5<<<112, 512, 0, stream>>>(hbuf, wg, wu, act);
    k6<<<32, 512, 0, stream>>>(hbuf, act, wd, (u16*)d_out);
}
```

```cpp
#include <hip/hip_runtime.h>
#include <hip/hip_bf16.h>
#include <stdint.h>

typedef unsigned short u16;
__device__ __forceinline__ float bf2f(u16 v) { return __uint_as_float(((unsigned)v) << 16); }
__device__ __forceinline__ u16 f2bf(float f) { unsigned u = __float_as_uint(f); u += 0x7fffu + ((u >> 16) & 1u); return (u16)(u >> 16); }

constexpr int B = 4, H = 32, SK = 4096, D = 128, M = 512, DC = 512, F = 1792;

__global__ void __launch_bounds__(512) k1(const u16* x, const u16* wq, const u16* wuk, float* qabs) {
    __shared__ float xn[B][M];
    __shared__ float q[B][D];
    int h = blockIdx.x, t = threadIdx.x;
    {
        int w = t >> 6, l = t & 63;
        if (w < B) {
            float s = 0.f, v[8];
            for (int i = 0; i < 8; ++i) { v[i] = bf2f(x[w * M + l * 8 + i]); s += v[i]; }
            for (int o = 32; o; o >>= 1) s += __shfl_xor(s, o);
            float mean = s / M, ss = 0.f;
            for (int i = 0; i < 8; ++i) { float d = v[i] - mean; ss += d * d; }
            for (int o = 32; o; o >>= 1) ss += __shfl_xor(ss, o);
            float r = rsqrtf(ss / M + 1e-5f);
            for (int i = 0; i < 8; ++i) xn[w][l * 8 + i] = (v[i] - mean) * r;
        }
    }
    __syncthreads();
    {
        int b = t >> 7, d = t & 127;
        float s = 0.f;
        for (int m = 0; m < M; ++m) s += xn[b][m] * bf2f(wq[(size_t)m * H * D + h * D + d]);
        q[b][d] = s;
    }
    __syncthreads();
    {
        int c = t;
        float s[B] = {0, 0, 0, 0};
        for (int d = 0; d < D; ++d) { float w = bf2f(wuk[(size_t)c * H * D + h * D + d]); for (int b = 0; b < B; ++b) s[b] += q[b][d] * w; }
        for (int b = 0; b < B; ++b) qabs[(b * H + h) * DC + c] = s[b] * 0.08838834764831845f;
    }
}
__global__ void __launch_bounds__(512) k2(const u16* ckv, const float* qabs, float* part, float* ml) {
    __shared__ float s[H][64];
    int b = blockIdx.x >> 6, sp = blockIdx.x & 63, t = threadIdx.x;
    const u16* kv = ckv + ((size_t)b * SK + sp * 64) * DC;
    for (int i = t; i < H * 64; i += 512) {
        int h = i >> 6, k = i & 63;
        float a = 0.f;
        for (int c = 0; c < DC; ++c) a += qabs[(b * H + h) * DC + c] * bf2f(kv[k * DC + c]);
        s[h][k] = a;
    }
    __syncthreads();
    if (t < H) {
        float m = -1e30f;
        for (int k = 0; k < 64; ++k) m = fmaxf(m, s[t][k]);
        float l = 0.f;
        for (int k = 0; k < 64; ++k) { float p = __expf(s[t][k] - m); s[t][k] = p; l += p; }
        ml[((b * H + t) * 64 + sp) * 2] = m; ml[((b * H + t) * 64 + sp) * 2 + 1] = l;
    }
    __syncthreads();
    for (int h = 0; h < H; ++h) {
        float a = 0.f;
        for (int k = 0; k < 64; ++k) a += s[h][k] * bf2f(kv[k * DC + t]);
        part[((size_t)(b * H + h) * 64 + sp) * DC + t] = a;
    }
}
__global__ void __launch_bounds__(512) k3(const float* part, const float* ml, const u16* wuv, float* ctx) {
    __shared__ float cl[DC];
    __shared__ float red[4][D];
    int bh = blockIdx.x, h = bh & 31, t = threadIdx.x;
    float m = -1e30f;
    for (int s = 0; s < 64; ++s) m = fmaxf(m, ml[(bh * 64 + s) * 2]);
    float l = 0.f, a = 0.f;
    for (int s = 0; s < 64; ++s) { float w = __expf(ml[(bh * 64 + s) * 2] - m); l += w * ml[(bh * 64 + s) * 2 + 1]; a += w * part[((size_t)bh * 64 + s) * DC + t]; }
    cl[t] = a / l;
    __syncthreads();
    int d = t & 127, g = t >> 7;
    float s = 0.f;
    for (int c = g * 128; c < g * 128 + 128; ++c) s += cl[c] * bf2f(wuv[(size_t)c * H * D + h * D + d]);
    red[g][d] = s;
    __syncthreads();
    if (t < D) ctx[bh * D + t] = red[0][t] + red[1][t] + red[2][t] + red[3][t];
}
__global__ void __launch_bounds__(512) k4(const u16* x, const float* ctx, const u16* wo, float* hbuf) {
    __shared__ float red[8][64];
    int b = blockIdx.x >> 3, n = (blockIdx.x & 7) * 64 + (threadIdx.x & 63), g = threadIdx.x >> 6;
    float s = 0.f;
    for (int k = g * 512; k < g * 512 + 512; ++k) s += ctx[b * H * D + k] * bf2f(wo[(size_t)k * M + n]);
    red[g][threadIdx.x & 63] = s;
    __syncthreads();
    if (g == 0) { float a = 0.f; for (int i = 0; i < 8; ++i) a += red[i][threadIdx.x]; hbuf[b * M + n] = bf2f(x[b * M + n]) + a; }
}
__global__ void __launch_bounds__(512) k5(const float* hbuf, const u16* wg, const u16* wu, float* act) {
    __shared__ float hn[M];
    __shared__ float rg[8][64], ru[8][64];
    int b = blockIdx.x / 28, f = (blockIdx.x % 28) * 64 + (threadIdx.x & 63), g = threadIdx.x >> 6, t = threadIdx.x;
    if (t < 64) {
        float s = 0.f, v[8];
        for (int i = 0; i < 8; ++i) { v[i] = hbuf[b * M + t * 8 + i]; s += v[i]; }
        for (int o = 32; o; o >>= 1) s += __shfl_xor(s, o);
        float mean = s / M, ss = 0.f;
        for (int i = 0; i < 8; ++i) { float d = v[i] - mean; ss += d * d; }
        for (int o = 32; o; o >>= 1) ss += __shfl_xor(ss, o);
        float r = rsqrtf(ss / M + 1e-5f);
        for (int i = 0; i < 8; ++i) hn[t * 8 + i] = (v[i] - mean) * r;
    }
    __syncthreads();
    float sg = 0.f, su = 0.f;
    for (int k = g * 64; k < g * 64 + 64; ++k) { sg += hn[k] * bf2f(wg[(size_t)k * F + f]); su += hn[k] * bf2f(wu[(size_t)k * F + f]); }
    rg[g][t & 63] = sg; ru[g][t & 63] = su;
    __syncthreads();
    if (g == 0) { float a = 0.f, c = 0.f; for (int i = 0; i < 8; ++i) { a += rg[i][t]; c += ru[i][t]; } act[b * F + f] = fmaxf(a, 0.f) * c; }
}
__global__ void __launch_bounds__(512) k6(const float* hbuf, const float* act, const u16* wd, u16* out) {
    __shared__ float red[8][64];
    int b = blockIdx.x >> 3, n = (blockIdx.x & 7) * 64 + (threadIdx.x & 63), g = threadIdx.x >> 6;
    float s = 0.f;
    for (int k = g * 224; k < g * 224 + 224; ++k) s += act[b * F + k] * bf2f(wd[(size_t)k * M + n]);
    red[g][threadIdx.x & 63] = s;
    __syncthreads();
    if (g == 0) { float a = 0.f; for (int i = 0; i < 8; ++i) a += red[i][threadIdx.x]; out[b * M + n] = f2bf(hbuf[b * M + n] + a); }
}

#define PROBE 6
#define NL 2

#define GAS __attribute__((address_space(1)))
typedef unsigned v4u __attribute__((ext_vector_type(4)));
__device__ __forceinline__ unsigned long long rt() { return __builtin_amdgcn_s_memrealtime(); }
#define R4(x) x x x x
#define R16(x) R4(R4(x))
#define R256(x) R16(R16(x))
#define BLK8K(v) asm volatile(R4(R256("v_add3_u32 %0, %0, %0, 1\n")) : "+v"(v))
__global__ void __launch_bounds__(512) k_probe(const v4u* __restrict__ src, unsigned char* wsp, unsigned* stamps) {
    extern __shared__ unsigned char lds[];
    const int wg = blockIdx.x;
    unsigned long long t0 = rt(), t1;
    unsigned acc = 0;
    const GAS v4u* p = (const GAS v4u*)src + (size_t)wg * (NL * 512) + threadIdx.x;
    v4u v[NL];
#pragma unroll
    for (int i = 0; i < NL; ++i) { if (PROBE == 2) v[i] = __builtin_nontemporal_load(p + i * 512); else v[i] = p[i * 512]; }
    v4u a = v[0];
#pragma unroll
    for (int i = 1; i < NL; ++i) a ^= v[i];
    asm volatile("" : "+v"(a));
    acc = a.x ^ a.y ^ a.z ^ a.w;
    __builtin_amdgcn_sched_barrier(0);
    t1 = rt();
    if ((threadIdx.x & 63) == 0) stamps[wg * 8 + (threadIdx.x >> 6)] = (unsigned)(t1 - t0) + (acc == 0x12345678u);
}
__global__ void __launch_bounds__(64) k_spin(const unsigned* stamps) {
    unsigned m = 0;
    for (int i = threadIdx.x; i < 2048; i += 64) m = max(m, stamps[i]);
    for (int o = 32; o; o >>= 1) m = max(m, (unsigned)__shfl_xor((int)m, o));
    if (m > 5000) m = 5000;
    unsigned long long t0 = rt();
    while (rt() < t0 + (unsigned long long)m * 100) __builtin_amdgcn_s_sleep(2);
}

extern "C" void kernel_launch(void* const* d_in, const int* in_sizes, int n_in,
                              void* d_out, int out_size, void* d_ws, size_t ws_size,
                              hipStream_t stream) {
    const u16* x = (const u16*)d_in[0];
    const u16* ckv = (const u16*)d_in[1];
    const u16* wq = (const u16*)d_in[2];
    const u16* wuk = (const u16*)d_in[3];
    const u16* wuv = (const u16*)d_in[4];
    const u16* wo = (const u16*)d_in[5];
    const u16* wg = (const u16*)d_in[7];
    const u16* wu = (const u16*)d_in[8];
    const u16* wd = (const u16*)d_in[9];
    float* ws = (float*)d_ws;
    float* qabs = ws;
    float* ml = qabs + B * H * DC;
    float* ctx = ml + B * H * 64 * 2;
    float* hbuf = ctx + B * H * D;
    float* act = hbuf + B * M;
    float* part = act + B * F + 1024;
    unsigned* stamps = (unsigned*)((char*)d_ws + (32u << 20));
    hipFuncSetAttribute((const void*)k_probe, hipFuncAttributeMaxDynamicSharedMemorySize, 96 * 1024);
    k_probe<<<256, 512, 96 * 1024, stream>>>((const v4u*)d_in[2], (unsigned char*)d_ws + (0), stamps);
    k1<<<32, 512, 0, stream>>>(x, wq, wuk, qabs);
    k2<<<256, 512, 0, stream>>>(ckv, qabs, part, ml);
    k3<<<128, 512, 0, stream>>>(part, ml, wuv, ctx);
    k4<<<32, 512, 0, stream>>>(x, ctx, wo, hbuf);
    k5<<<112, 512, 0, stream>>>(hbuf, wg, wu, act);
    k6<<<32, 512, 0, stream>>>(hbuf, act, wd, (u16*)d_out);
    k_spin<<<1, 64, 0, stream>>>(stamps);
}
```

```cpp
#define STAMPS
#define UK_SLEEP "6"
#define KV_SLEEP 14
#include <hip/hip_runtime.h>
#include <stdint.h>

#define GAS __attribute__((address_space(1)))
#define LAS __attribute__((address_space(3)))
typedef unsigned short u16;
typedef unsigned v4u __attribute__((ext_vector_type(4)));
typedef unsigned v2u __attribute__((ext_vector_type(2)));
typedef float f32x4 __attribute__((ext_vector_type(4)));
typedef float f32x2 __attribute__((ext_vector_type(2)));
typedef float f32x16 __attribute__((ext_vector_type(16)));
typedef short s16x4 __attribute__((ext_vector_type(4)));
typedef __bf16 bf16x8 __attribute__((ext_vector_type(8)));
typedef __bf16 bf16x2 __attribute__((ext_vector_type(2)));

#define KNOB(n, v)
#define NT_POLICY " nt"
#define GU_POLICY ""
#define HOP1_SLEEP 6
#define STAT_SLEEP 16
#define Q_SLEEP 0
#define TAIL_SLEEP 60
#define FFN_SLEEP 40
#define PART_SLEEP 10
#define H4_SLEEP 10
#define H5_SLEEP 12
#define H6_SLEEP 10
#define POISON_PART 3
#define CODE_BYTES "17600"

constexpr unsigned W_EPOCH = 0;
constexpr unsigned W_HINT = 4096;
constexpr unsigned W_QP = W_HINT + 32768;
constexpr unsigned W_QABS = W_QP + 262144;
constexpr unsigned W_STAT = W_QABS + 196608;
constexpr unsigned W_CTXP = W_STAT + 65536;
constexpr unsigned W_OUTP = W_CTXP + 131072;
constexpr unsigned W_HP = W_OUTP + 56 * 4096;
constexpr unsigned W_PART = 1u << 20;
constexpr unsigned W_STAMP = W_PART + (4u << 20);
constexpr unsigned W_BYTES = 6u << 20;
static_assert(W_OUTP + 64 * 4096 <= W_PART, "arena");
constexpr int LDS_BYTES = 163840;
constexpr unsigned POISON = 0xFFFFFFFFu;
constexpr int SPIN_CAP = 1 << 16;

__device__ __forceinline__ unsigned pk2(float a, float b) { f32x2 v = {a, b}; return __builtin_bit_cast(unsigned, __builtin_convertvector(v, bf16x2)); }
__device__ __forceinline__ float bflo(unsigned u) { return __uint_as_float(u << 16); }
__device__ __forceinline__ float bfhi(unsigned u) { return __uint_as_float(u & 0xFFFF0000u); }
__device__ __forceinline__ f32x4 mfma16(v4u a, v4u b, f32x4 c) { return __builtin_amdgcn_mfma_f32_16x16x32_bf16(__builtin_bit_cast(bf16x8, a), __builtin_bit_cast(bf16x8, b), c, 0, 0, 0); }
__device__ __forceinline__ f32x16 mfma32(v4u a, v4u b, f32x16 c) { return __builtin_amdgcn_mfma_f32_32x32x16_bf16(__builtin_bit_cast(bf16x8, a), __builtin_bit_cast(bf16x8, b), c, 0, 0, 0); }
__device__ __forceinline__ v4u lds128(unsigned a) { return *(LAS v4u*)(uintptr_t)a; }
__device__ __forceinline__ v2u ldstr(unsigned a) { return __builtin_bit_cast(v2u, __builtin_amdgcn_ds_read_tr16_b64_v4i16((LAS s16x4*)(uintptr_t)a)); }
__device__ __forceinline__ v4u ldstr2(unsigned a0, unsigned a1) { v2u x = ldstr(a0), y = ldstr(a1); return (v4u){x.x, x.y, y.x, y.y}; }
__device__ __forceinline__ void sts128(unsigned a, v4u v) { *(LAS v4u*)(uintptr_t)a = v; }
__device__ __forceinline__ void sts64(unsigned a, v2u v) { *(LAS v2u*)(uintptr_t)a = v; }
__device__ __forceinline__ void sts32(unsigned a, float v) { *(LAS float*)(uintptr_t)a = v; }
__device__ __forceinline__ float lds32(unsigned a) { return *(LAS float*)(uintptr_t)a; }
#define BAR() asm volatile("s_waitcnt lgkmcnt(0)\n\ts_barrier" ::: "memory")
#define VMWAIT(n) asm volatile("s_waitcnt vmcnt(" #n ")" ::: "memory")
#define CFENCE() asm volatile("" ::: "memory")
typedef __amdgpu_buffer_rsrc_t rsrc_t;
__device__ __forceinline__ v4u gld128(rsrc_t r, unsigned off, unsigned so = 0) { return __builtin_amdgcn_raw_buffer_load_b128(r, off, so, 16); }
__device__ __forceinline__ v2u gld64(rsrc_t r, unsigned off) { return __builtin_amdgcn_raw_buffer_load_b64(r, off, 0, 16); }
__device__ __forceinline__ void gst128(rsrc_t r, unsigned off, v4u v, unsigned so = 0) { __builtin_amdgcn_raw_buffer_store_b128(v, r, off, so, 16); }
__device__ __forceinline__ void gst64(rsrc_t r, unsigned off, v2u v) { __builtin_amdgcn_raw_buffer_store_b64(v, r, off, 0, 16); }
__device__ __forceinline__ void gst32(rsrc_t r, unsigned off, unsigned v) { __builtin_amdgcn_raw_buffer_store_b32(v, r, off, 0, 16); }
__device__ __forceinline__ v4u mksrd(const void* p, unsigned bytes) { const uint64_t a = (uint64_t)p; return (v4u){(unsigned)a, (unsigned)(a >> 32) & 0xFFFFu, bytes, 0x00020000u}; }
__device__ __forceinline__ void dma(v4u srd, unsigned voff, unsigned soff, unsigned dst) {
    asm volatile("s_mov_b32 m0, %3\n\ts_nop 0\n\tbuffer_load_dwordx4 %0, %1, %2 offen" NT_POLICY " lds" :: "v"(voff), "s"(srd), "s"(soff), "s"(dst) : "memory");
}
__device__ __forceinline__ void dmag(const void* p, unsigned dst) {
    asm volatile("s_mov_b32 m0, %1\n\ts_nop 0\n\tglobal_load_lds_dwordx4 %0, off" GU_POLICY :: "v"(p), "s"(dst) : "memory");
}
__device__ __forceinline__ void dma_loop(v4u srd, unsigned voff, unsigned soff, unsigned sstep, unsigned dst, unsigned dstep, unsigned n) {
    asm volatile("s_mov_b32 m0, %4\n\ts_nop 0\n"
                 ".Ldl%=:\n\t"
                 "buffer_load_dwordx4 %2, %3, %0 offen" NT_POLICY " lds\n\t"
                 "s_add_u32 m0, m0, %6\n\ts_add_u32 %0, %0, %5\n\ts_sub_u32 %1, %1, 1\n\ts_cmp_lg_u32 %1, 0\n\ts_cbranch_scc1 .Ldl%="
                 : "+s"(soff), "+s"(n) : "v"(voff), "s"(srd), "s"(dst), "s"(sstep), "s"(dstep) : "scc", "memory");
}
__device__ __forceinline__ unsigned umax4(v4u v, unsigned m) { return max(max(max(v.x, v.y), max(v.z, v.w)), m); }
__device__ __forceinline__ float xsum16(float v) { auto r = __builtin_amdgcn_permlane16_swap(__float_as_uint(v), __float_as_uint(v), false, false); return __uint_as_float(r[0]) + __uint_as_float(r[1]); }
__device__ __forceinline__ float xsum32(float v) { auto r = __builtin_amdgcn_permlane32_swap(__float_as_uint(v), __float_as_uint(v), false, false); return __uint_as_float(r[0]) + __uint_as_float(r[1]); }
__device__ __forceinline__ float xmax16(float v) { auto r = __builtin_amdgcn_permlane16_swap(__float_as_uint(v), __float_as_uint(v), false, false); return fmaxf(__uint_as_float(r[0]), __uint_as_float(r[1])); }
__device__ __forceinline__ float xmax32(float v) { auto r = __builtin_amdgcn_permlane32_swap(__float_as_uint(v), __float_as_uint(v), false, false); return fmaxf(__uint_as_float(r[0]), __uint_as_float(r[1])); }
template <int CTRL> __device__ __forceinline__ float dpp(float v) { return __int_as_float(__builtin_amdgcn_update_dpp(0, __float_as_int(v), CTRL, 0xf, 0xf, true)); }
__device__ __forceinline__ float rsum16(float v) { v += dpp<0xB1>(v); v += dpp<0x4E>(v); v += dpp<0x141>(v); v += dpp<0x140>(v); return v; }
__device__ __forceinline__ float rmax16(float v) { v = fmaxf(v, dpp<0xB1>(v)); v = fmaxf(v, dpp<0x4E>(v)); v = fmaxf(v, dpp<0x141>(v)); v = fmaxf(v, dpp<0x140>(v)); return v; }
__device__ __forceinline__ float wsum(float v) { return xsum32(xsum16(rsum16(v))); }

#define STAMP(k) do { __builtin_amdgcn_sched_barrier(0); unsigned long long t_; asm volatile("s_memrealtime %0\n\ts_waitcnt lgkmcnt(0)" : "=s"(t_) :: "memory"); \
    stv[k] = (unsigned)t_; __builtin_amdgcn_sched_barrier(0); } while (0)
#define STAMP_OUT() do { if (l == 0) { GAS unsigned* sp_ = (GAS unsigned*)(ws + W_STAMP) + (wg * 8 + w) * 16; _Pragma("unroll") for (int k_ = 0; k_ < 16; ++k_) sp_[k_] = stv[k_]; } } while (0)

__device__ __forceinline__ v4u selmask(int l, int r0) {
    const int j = (l & 15) - r0;
    const unsigned m = (j & 1) ? 0xFFFF0000u : 0x0000FFFFu;
    return (v4u){(j >> 1) == 0 ? m : 0u, (j >> 1) == 1 ? m : 0u, (j >> 1) == 2 ? m : 0u, (j >> 1) == 3 ? m : 0u};
}

__device__ __forceinline__ unsigned gld32(rsrc_t r, unsigned off) { return __builtin_amdgcn_raw_buffer_load_b32(r, off, 0, 16); }
template <int SLEEP> __device__ __forceinline__ void wait_flag(rsrc_t rs, unsigned off, unsigned tag) {
    _Pragma("unroll 1") for (int it = 0; it < SPIN_CAP; ++it) {
        CFENCE();
        const unsigned v = gld32(rs, off);
        if (__all(v == tag)) break;
        __builtin_amdgcn_s_sleep(SLEEP);
    }
    CFENCE();
}
#define SB() __builtin_amdgcn_sched_barrier(0)
template <int N> __device__ __forceinline__ void gather(rsrc_t rs, unsigned off, unsigned stride, v4u (&v)[N]) {
    _Pragma("unroll 1") for (int it = 0; it < SPIN_CAP; ++it) {
        CFENCE();
        unsigned m = 0;
#pragma unroll
        for (int k = 0; k < N; ++k) v[k] = gld128(rs, off, k * stride);
#pragma unroll
        for (int k = 0; k < N; ++k) m = umax4(v[k], m);
        if (__all(m != POISON)) break;
    }
    CFENCE();
}

__global__ void __launch_bounds__(512, 2)
fused_decode(const u16* __restrict__ wq, const u16* __restrict__ wuk, const u16* __restrict__ ckv_arg, const u16* __restrict__ x,
             unsigned char* __restrict__ ws_arg, const u16* __restrict__ wuv, const u16* __restrict__ wo, const u16* __restrict__ wg_,
             const u16* __restrict__ wu_, const u16* __restrict__ wd, u16* __restrict__ out) {
    extern __shared__ __attribute__((aligned(16))) unsigned char lds_[];
    unsigned stv[16];
#pragma unroll
    for (int k = 0; k < 16; ++k) stv[k] = 0;
    STAMP(0);
    v4u xv;
    unsigned rawtag;
    unsigned char* ws;
    const u16* ckv;
    {
        unsigned t0, t1, t2, t3, pfs;
#define PDMA(V) "buffer_load_dwordx4 " V ", s[52:55], s58 offen" NT_POLICY " lds\n\t"
#define PSTEP "s_add_u32 m0, m0, 0x400\n\ts_add_u32 s58, s58, 0x1000\n\t"
        asm volatile(
            "s_load_dwordx8 s[40:47], %[ka], 0x0\n\t"
            "s_load_dwordx2 %[ws], %[ka], 0x20\n\t"
            "v_readfirstlane_b32 s50, %[tid]\n\t"
            "s_bfe_u32 s51, %[wg], 0x40003\n\t"
            "s_lshr_b32 s50, s50, 6\n\t"
            "s_cmp_lt_u32 s50, 4\n\t"
            "s_cbranch_scc1 .Ldm%=\n\t"
            "s_cmp_lt_u32 %[wg], 0x80\n\t"
            "s_cbranch_scc1 .Lxl%=\n\t"
            "s_getpc_b64 s[56:57]\n\t"
            "s_lshl_b32 s60, s51, 2\n\t"
            "s_add_u32 s60, s60, s50\n\t"
            "s_mul_i32 s60, s60, 320\n\t"
            "s_sub_u32 s60, s60, 1280\n\t"
            "s_min_u32 s60, s60, " CODE_BYTES "\n\t"
            "s_add_u32 s56, s56, s60\n\t"
            "s_addc_u32 s57, s57, 0\n\t"
            "s_lshl_b32 s58, %[wg], 15\n\t"
            "s_add_u32 s58, s58, 0xffcff000\n\t"
            "v_lshl_add_u32 %[t0], %[tid], 4, s58\n\t"
            "v_mov_b32 %[t1], -1\n\t"
            "s_mov_b32 s54, 0x600000\n\t"
            "s_mov_b32 s55, 0x20000\n\t"
            "s_mov_b32 s58, 0\n\t"
            "s_waitcnt lgkmcnt(0)\n\t"
            "s_mov_b64 s[52:53], %[ws]\n\t"
            "s_and_b32 s53, s53, 0xffff\n\t"
            "s_load_dword %[pf], s[56:57], 0x0\n\t"
            "s_load_dword %[pf], s[56:57], 0x40\n\t"
            "s_load_dword %[pf], s[56:57], 0x80\n\t"
            "s_load_dword %[pf], s[56:57], 0xc0\n\t"
            "s_load_dword %[pf], s[56:57], 0x100\n"
            ".Lpz%=:\n\t"
            "buffer_store_dwordx4 %[pz], %[t0], s[52:55], s58 offen sc1\n\t"
            "s_add_u32 s58, s58, 0x1000\n\t"
            "s_cmp_lg_u32 s58, 0x8000\n\t"
            "s_cbranch_scc1 .Lpz%=\n\t"
            "s_waitcnt lgkmcnt(0)\n\t"
            "s_load_dword %[tag], %[ws], 0x0\n\t"
            "s_branch .Lpe%=\n"
            ".Lxl%=:\n\t"
            "v_lshlrev_b32 %[t1], 4, %[tid]\n\t"
            "s_waitcnt lgkmcnt(0)\n\t"
            "global_load_dwordx4 %[xv], %[t1], s[46:47] offset:-4096\n\t"
            "s_load_dword %[tag], %[ws], 0x0\n\t"
            "s_branch .Lpe%=\n"
            ".Ldm%=:\n\t"
            "v_bfe_u32 %[t2], %[tid], 4, 2\n\t"
            "v_and_b32 %[t3], 15, %[tid]\n\t"
            "s_and_b32 s59, %[wg], 7\n\t"
            "s_lshl_b32 s62, s50, 15\n\t"
            "s_lshl_b32 s58, s59, 10\n\t"
            "s_lshr_b32 s60, s51, 2\n\t"
            "s_lshl_b32 s60, s60, 20\n\t"
            "s_and_b32 s61, s51, 3\n\t"
            "s_lshl_b32 s61, s61, 8\n\t"
            "s_add_u32 s58, s58, s62\n\t"
            "s_add_u32 s60, s60, s61\n\t"
            "s_add_u32 s58, s58, s60\n\t"
            "v_lshlrev_b32 %[t0], 2, %[t2]\n\t"
            "v_xor_b32 %[t1], s50, %[t3]\n\t"
            "v_xor_b32 %[t1], %[t0], %[t1]\n\t"
            "v_lshlrev_b32 %[t0], 13, %[t2]\n\t"
            "v_lshl_add_u32 %[t0], %[t1], 4, %[t0]\n\t"
            "s_lshl_b32 s60, s50, 10\n\t"
            "s_add_u32 m0, s60, 0x20000\n\t"
            "s_mov_b32 s54, 0x400000\n\t"
            "s_mov_b32 s55, 0x20000\n\t"
            "s_mov_b32 s61, 8\n\t"
            "s_cmp_lt_u32 %[wg], 0x80\n\t"
            "s_waitcnt lgkmcnt(0)\n\t"
            "s_cselect_b32 s52, s40, s42\n\t"
            "s_cselect_b32 s53, s41, s43\n\t"
            "s_and_b32 s53, s53, 0xffff\n\t"
            "s_mov_b64 %[kv], s[44:45]\n\t"
            "s_load_dword %[tag], %[ws], 0x0\n\t"
            "s_cmp_lt_u32 %[wg], 0x80\n\t"
            "s_cbranch_scc1 .Lf1%=\n\t"
            "s_sleep " UK_SLEEP "\n"
            ".Lf1%=:\n\t"
            PDMA("%[t0]")
            "s_add_u32 m0, m0, 0x1000\n\ts_add_u32 s58, s58, 0x20000\n\ts_sub_u32 s61, s61, 1\n\ts_cmp_lg_u32 s61, 0\n\ts_cbranch_scc1 .Lf1%=\n"
            ".Lpe%=:"
            : [xv] "=&v"(xv), [tag] "=&s"(rawtag), [ws] "=&s"(ws), [kv] "=&s"(ckv), [pf] "=&s"(pfs), [t0] "=&v"(t0), [t1] "=&v"(t1), [t2] "=&v"(t2), [t3] "=&v"(t3)
            : [ka] "s"(__builtin_amdgcn_kernarg_segment_ptr()), [tid] "v"(threadIdx.x), [wg] "s"(blockIdx.x), [pz] "v"((v4u){POISON, POISON, POISON, POISON})
            : "s40", "s41", "s42", "s43", "s44", "s45", "s46", "s47", "s50", "s51", "s52", "s53", "s54", "s55", "s56", "s57", "s58", "s59", "s60", "s61", "s62", "scc", "memory");
    }
    SB();
    const int wg = blockIdx.x, l = threadIdx.x & 63, w = __builtin_amdgcn_readfirstlane(threadIdx.x >> 6);
    const int xc = wg & 7, ix = (wg >> 3) & 15;
    const int g = l >> 4, p16 = l & 15;
    const int hh = 4 * xc + (ix & 3), qd = ix >> 2;
    const int ab = xc >> 1, arow = xc * 16 + ix;
    unsigned tag;
#define GET_TAG() asm volatile("s_waitcnt lgkmcnt(0)\n\ts_add_u32 %0, %1, 0x01010101" : "=s"(tag) : "s"(rawtag) : "scc", "memory")
    const rsrc_t rs = __builtin_amdgcn_make_buffer_rsrc(ws, 0, W_BYTES, 0x00020000);
    const unsigned trq = (unsigned)(p16 >> 2), trp = (unsigned)(l & 3);
    const unsigned tra = (8 * g + trq) * 256 + (((2 * w + (trp >> 1)) ^ ((trq << 2) | ((2 * g) & 3))) * 16) + 8 * (trp & 1);

    if (__builtin_expect(wg < 128, 1)) {
        constexpr unsigned L_Q = 131072, L_XN = 98304, L_MX = 0, L_LS = 512, L_P = 1024, L_CL = 12288, L_WUV = 32768;
        auto kvload = [&](unsigned cb, unsigned n) {
            const unsigned v0 = (unsigned)g * 1024u + (unsigned)((p16 ^ (2 * g)) * 16);
            unsigned so = (unsigned)(arow * 128 + 32 * w) * 1024u + cb * 256;
#define KDMA(V) "buffer_load_dwordx4 " V ", %6, %0 offen" NT_POLICY " lds\n\t"
#define KSTEP "s_add_u32 m0, m0, 0x400\n\ts_add_u32 %0, %0, 0x1000\n\t"
            asm volatile("s_mov_b32 m0, %7\n\ts_nop 0\n"
                         ".Lkv%=:\n\t"
                         KDMA("%2") KSTEP KDMA("%3") KSTEP KDMA("%4") KSTEP KDMA("%5") KSTEP KDMA("%2") KSTEP KDMA("%3") KSTEP KDMA("%4") KSTEP KDMA("%5")
                         "s_add_u32 m0, m0, 25600\n\ts_add_u32 %0, %0, 0xffff9100\n\ts_sub_u32 %1, %1, 1\n\ts_cmp_lg_u32 %1, 0\n\ts_cbranch_scc1 .Lkv%="
                         : "+s"(so), "+s"(n) : "v"(v0), "v"(v0 ^ 128u), "v"(v0 ^ 16u), "v"(v0 ^ 144u), "s"(mksrd(ckv, 16777216u)), "s"(cb * 32768 + w * 8192) : "scc", "memory");
        };
        if (w < 4) {
            asm volatile("s_barrier" ::: "memory");
            __builtin_amdgcn_s_sleep(KV_SLEEP);
            kvload(0, 1);
            GET_TAG();
            STAMP(1);
            VMWAIT(8);
        } else {
            asm volatile("s_barrier" ::: "memory");
            GET_TAG();
            STAMP(1);
            asm volatile("s_waitcnt vmcnt(0)" : "+v"(xv) :: "memory");
            float v[8] = {bflo(xv.x), bfhi(xv.x), bflo(xv.y), bfhi(xv.y), bflo(xv.z), bfhi(xv.z), bflo(xv.w), bfhi(xv.w)};
            const float mean = wsum(((v[0] + v[1]) + (v[2] + v[3])) + ((v[4] + v[5]) + (v[6] + v[7]))) * (1.f / 512.f);
            float ss = 0.f;
#pragma unroll
            for (int j = 0; j < 8; ++j) { v[j] -= mean; ss += v[j] * v[j]; }
            const float rstd = rsqrtf(wsum(ss) * (1.f / 512.f) + 1e-5f);
            if (g == qd) sts128(L_XN + (w - 4) * 320 + p16 * 16, (v4u){pk2(v[0] * rstd, v[1] * rstd), pk2(v[2] * rstd, v[3] * rstd), pk2(v[4] * rstd, v[5] * rstd), pk2(v[6] * rstd, v[7] * rstd)});
        }
        STAMP(12);
        BAR();
        {
            v4u af[4], bf[4];
#pragma unroll
            for (int s = 0; s < 4; ++s) { af[s] = ldstr2(L_Q + tra + s * 8192, L_Q + (tra ^ 16) + 1024 + s * 8192); bf[s] = lds128(L_XN + (l & 3) * 320 + g * 16 + s * 64); }
            SB();
            f32x4 acc = {0.f, 0.f, 0.f, 0.f};
#pragma unroll
            for (int s = 0; s < 4; ++s) acc = mfma16(af[s], bf[s], acc);
            v4u o;
            o.x = (__float_as_uint(acc.x) & ~255u) | (tag & 255u); o.y = (__float_as_uint(acc.y) & ~255u) | ((tag >> 8) & 255u);
            o.z = (__float_as_uint(acc.z) & ~255u) | ((tag >> 16) & 255u); o.w = (__float_as_uint(acc.w) & ~255u) | (tag >> 24);
            gst128(rs, p16 < 4 ? W_QP + (unsigned)(((hh * 4 + qd) * 8 + w) * 256 + p16 * 64 + g * 16) : W_BYTES, o);
        }
        STAMP(13);
        BAR();
        v4u uf[4];
        if (w < 4) {
            kvload(1, 3);
            STAMP(14);
            const int r = l & 31, hf = l >> 5;
            const unsigned fa = (unsigned)(r * 256 + ((hf ^ (((r & 7) << 1) | ((r >> 3) & 1))) * 16));
            v4u kf[32];
            VMWAIT(16);
#pragma unroll
            for (int s = 0; s < 16; ++s) kf[s] = lds128((fa ^ ((s & 7) * 32)) + (s >> 3) * 32768 + w * 8192);
            BAR();
            STAMP(2);
            VMWAIT(8);
#pragma unroll
            for (int s = 16; s < 24; ++s) kf[s] = lds128((fa ^ ((s & 7) * 32)) + (s >> 3) * 32768 + w * 8192);
            VMWAIT(0);
            STAMP(3);
#pragma unroll
            for (int s = 24; s < 32; ++s) kf[s] = lds128((fa ^ ((s & 7) * 32)) + (s >> 3) * 32768 + w * 8192);
            BAR();
            STAMP(4);
            f32x16 sa;
#pragma unroll
            for (int j = 0; j < 16; ++j) sa[j] = 0.f;
            {
                v4u qf[32];
#pragma unroll
                for (int s = 0; s < 12; ++s) qf[s] = lds128(L_Q + (fa ^ ((s & 7) * 32)) + (s >> 3) * 8192);
                SB();
#pragma unroll
                for (int s = 0; s < 32; ++s) {
                    sa = mfma32(kf[s], qf[s], sa);
                    if (s + 12 < 32) qf[s + 12] = lds128(L_Q + (fa ^ (((s + 12) & 7) * 32)) + ((s + 12) >> 3) * 8192);
                    SB();
                }
            }
            float m = sa[0];
#pragma unroll
            for (int j = 1; j < 16; ++j) m = fmaxf(m, sa[j]);
            m = xmax32(m);
            if (hf == 0) sts32(L_MX + r * 16 + w * 4, m);
            STAMP(5);
            BAR();
            const f32x4 mv = __builtin_bit_cast(f32x4, lds128(L_MX + r * 16));
            const float M = fmaxf(fmaxf(mv.x, mv.y), fmaxf(mv.z, mv.w));
            unsigned pw[8];
#pragma unroll
            for (int j = 0; j < 8; ++j) pw[j] = pk2(__builtin_amdgcn_exp2f(sa[2 * j] - M), __builtin_amdgcn_exp2f(sa[2 * j + 1] - M));
            const unsigned pa = L_P + (unsigned)((w * 2 + (r >> 4)) * 1024 + hf * 256 + (r & 15) * 16);
            sts128(pa, (v4u){pw[0], pw[1], pw[2], pw[3]});
            sts128(pa + 512, (v4u){pw[4], pw[5], pw[6], pw[7]});
            float ls = 0.f;
#pragma unroll
            for (int j = 0; j < 8; ++j) ls += bflo(pw[j]) + bfhi(pw[j]);
            ls = xsum32(ls);
            if (hf == 0) sts32(L_LS + r * 16 + w * 4, ls);
            BAR();
            STAMP(6);
            if (w == 0 && l < 32) {
                const f32x4 lv = __builtin_bit_cast(f32x4, lds128(L_LS + r * 16));
                gst128(rs, W_STAT + (unsigned)(arow * 512 + r * 16), (v4u){__float_as_uint(M), tag, __float_as_uint((lv.x + lv.y) + (lv.z + lv.w)), tag});
            }
            const unsigned sto = W_STAT + (unsigned)((w * 32 + r) * 512 + hh * 16), pvo = W_PART + (unsigned)(((hh * 4 + qd) * 128 + w * 32) * 256 + l * 16);
            const v4u sm = selmask(l, 0);
            BAR();
            STAMP(7);
            v4u st = gld128(rs, sto);
#pragma unroll
            for (int s = 0; s < 4; ++s) uf[s] = ldstr2(L_WUV + tra + s * 8192, L_WUV + (tra ^ 16) + 1024 + s * 8192);
            SB();
            __builtin_amdgcn_s_sleep(PART_SLEEP);
            SB();
            v4u pv[8];
#pragma unroll
            for (int k = 0; k < 8; ++k) pv[k] = gld128(rs, pvo, k * 1024);
            SB();
            if (!__all(st.y == tag && st.w == tag)) {
                _Pragma("unroll 1") for (int it = 0; it < SPIN_CAP; ++it) {
                    CFENCE();
                    st = gld128(rs, sto);
                    if (__all(st.y == tag && st.w == tag)) break;
                }
            }
            STAMP(8);
            const float ms = __uint_as_float(st.x);
            const float Mx = xmax16(rmax16(ms));
            const unsigned cb2 = pk2(__builtin_amdgcn_exp2f(ms - Mx), __builtin_amdgcn_exp2f(ms - Mx));
            const float rl = 1.f / xsum16(rsum16(bflo(cb2) * __uint_as_float(st.z)));
            v4u cf[8];
#pragma unroll
            for (int t = 0; t < 8; ++t) { const unsigned c = (unsigned)__builtin_amdgcn_ds_bpermute((4 * t + g) * 4, (int)cb2); cf[t] = sm & c; }
            SB();
            {
                unsigned mx = 0;
#pragma unroll
                for (int k = 0; k < 8; ++k) mx = umax4(pv[k], mx);
                STAMP(15);
                if (!__all(mx != POISON)) gather<8>(rs, pvo, 1024, pv);
            }
            STAMP(9);
            if (w == 0 && l == 0) gst64(rs, W_HINT + wg * 128, (v2u){tag, tag});
            f32x4 acc = {0.f, 0.f, 0.f, 0.f};
#pragma unroll
            for (int t = 0; t < 8; ++t) acc = mfma16(cf[t], pv[t], acc);
            if (g < 2) sts64(L_CL + w * 320 + p16 * 16 + g * 8, (v2u){pk2(acc.x * rl, acc.y * rl), pk2(acc.z * rl, acc.w * rl)});
        } else {
            const int ow = w - 4;
            const unsigned va = (unsigned)(ow * 32768 + (16 * (g >> 1) + 4 * (g & 1) + trq) * 256 + (((trp >> 1) ^ (8 * (g & 1) + 2 * trq)) * 16) + 8 * (trp & 1));
            const unsigned po = W_PART + (unsigned)(((16 * g + ow) * 128 + arow) * 256 + p16 * 16);
            v4u vf[4][8];
            auto vread = [&]() {
#pragma unroll
                for (int kg = 0; kg < 4; ++kg)
#pragma unroll
                    for (int ct = 0; ct < 8; ++ct) vf[kg][ct] = ldstr2((va ^ (ct * 32)) + kg * 8192, (va ^ (ct * 32 + 16)) + kg * 8192 + 2048);
            };
            {
                const unsigned off = W_QABS + (unsigned)((ab * 32 + 8 * ow) * 1536 + (l >> 3) * 384 + (l & 7) * 16);
                const unsigned qa = L_Q + (unsigned)(((l >> 3) & 3) * 8192 + (8 * ow + (l >> 5)) * 256 + (((2 * (l & 7)) ^ (((l >> 5) << 1) | (ow & 1))) * 16));
                BAR();
                STAMP(2);
                __builtin_amdgcn_s_sleep(Q_SLEEP);
                _Pragma("unroll 1") for (int it = 0;; ++it) {
                    CFENCE();
                    v4u qv[4][3];
#pragma unroll
                    for (int k = 0; k < 4; ++k)
#pragma unroll
                        for (int p = 0; p < 3; ++p) qv[k][p] = gld128(rs, off + p * 128, k * 3072);
                    SB();
                    if (ow < 2 && it == 0) vread();
                    unsigned d = 0;
#pragma unroll
                    for (int k = 0; k < 4; ++k) {
                        const unsigned a = (qa ^ (k * 64)) + k * 512;
                        sts128(a, (v4u){qv[k][0].x, qv[k][0].z, __builtin_amdgcn_perm(qv[k][0].w, qv[k][0].y, 0x05040100u), qv[k][1].x});
                        sts128(a ^ 16, (v4u){qv[k][1].z, __builtin_amdgcn_perm(qv[k][1].w, qv[k][1].y, 0x05040100u), qv[k][2].x, qv[k][2].z});
#pragma unroll
                        for (int p = 0; p < 3; ++p) d |= (qv[k][p].y ^ (tag << 16)) | (qv[k][p].w ^ tag);
                    }
                    if (it == 0) STAMP(3);
                    if (__all(d < 65536u) || it >= SPIN_CAP) break;
                }
                STAMP(4);
                BAR();
            }
            if (ow >= 2) {
                vread();
            } else {
                const v4u srd = mksrd(wuv, 4194304);
                const unsigned vo = (unsigned)g * (16u * 8192u) + (unsigned)((p16 ^ (g << 2) ^ ow) * 16), so = (unsigned)(128 * qd + 64 * ow) * 8192u + hh * 256;
                dma_loop(srd, vo, so, 16384, L_WUV + ow * 1024, 4096, 8);
                dma_loop(srd, vo ^ 32, so + 8192, 16384, L_WUV + (2 + ow) * 1024, 4096, 8);
            }
            STAMP(5);
            BAR();
            if (ow >= 2) VMWAIT(0);
            STAMP(14);
            BAR();
            STAMP(6);
            v4u pf[8];
#pragma unroll
            for (int k = 0; k < 8; ++k) pf[k] = lds128(L_P + ((k & 3) * 2 + (k >> 2)) * 1024 + l * 16);
            SB();
            f32x4 acc[2][8];
#pragma unroll
            for (int ct = 0; ct < 8; ++ct) { acc[0][ct] = (f32x4){0.f, 0.f, 0.f, 0.f}; acc[1][ct] = (f32x4){0.f, 0.f, 0.f, 0.f}; }
#pragma unroll
            for (int kg = 0; kg < 4; ++kg)
#pragma unroll
                for (int ct = 0; ct < 8; ++ct) acc[0][ct] = mfma16(pf[kg], vf[kg][ct], acc[0][ct]);
            SB();
#pragma unroll
            for (int kg = 0; kg < 4; ++kg) {
#pragma unroll
                for (int ct = 0; ct < 8; ++ct) acc[1][ct] = mfma16(pf[4 + kg], vf[kg][ct], acc[1][ct]);
                gst128(rs, po, (v4u){pk2(acc[0][0][kg], acc[0][1][kg]), pk2(acc[0][2][kg], acc[0][3][kg]), pk2(acc[0][4][kg], acc[0][5][kg]), pk2(acc[0][6][kg], acc[0][7][kg])}, kg * 131072);
                SB();
            }
            {
                v4u o[4];
#pragma unroll
                for (int j = 0; j < 4; ++j) o[j] = (v4u){pk2(acc[1][0][j], acc[1][1][j]), pk2(acc[1][2][j], acc[1][3][j]), pk2(acc[1][4][j], acc[1][5][j]), pk2(acc[1][6][j], acc[1][7][j])};
                asm volatile("" : "+v"(o[0]), "+v"(o[1]), "+v"(o[2]), "+v"(o[3]));
                STAMP(8);
#pragma unroll
                for (int j = 0; j < 4; ++j) gst128(rs, po, o[j], 2097152 + j * 131072);
            }
            STAMP(7);
            VMWAIT(8);
            BAR();
#pragma unroll
            for (int s = 0; s < 4; ++s) uf[s] = ldstr2(L_WUV + tra + s * 8192, L_WUV + (tra ^ 16) + 1024 + s * 8192);
        }
        {
            BAR();
            v4u bf[4];
#pragma unroll
            for (int s = 0; s < 4; ++s) bf[s] = lds128(L_CL + (l & 3) * 320 + g * 16 + s * 64);
            SB();
            f32x4 acc = {0.f, 0.f, 0.f, 0.f};
#pragma unroll
            for (int s = 0; s < 4; ++s) acc = mfma16(uf[s], bf[s], acc);
            if (p16 < 4) gst64(rs, W_CTXP + (unsigned)((hh * 4 + qd) * 1024 + w * 128 + p16 * 32 + g * 8), (v2u){pk2(acc.x, acc.y), pk2(acc.z, acc.w)});
        }
        STAMP(10);
        if (ix < 4 && w == 4) {
            const int r = xc * 4 + ix, oi = ((l >> 1) & 3) * 512 + r * 16 + (l & 1) * 8 + (g & 1) * 4;
            const v2u xr = *(const GAS v2u*)(x + oi);
            const v4u sel0 = selmask(l, 0) & 0x3F803F80u;
            wait_flag<1>(rs, W_HINT + (unsigned)(wg + 128 + 64) * 128u, tag);
            __builtin_amdgcn_s_sleep(H6_SLEEP);
            STAMP(9);
            v4u v[8];
            gather<8>(rs, W_OUTP + (unsigned)((l >> 3) * 4096 + r * 128 + (l & 7) * 16), 8 * 4096, v);
            STAMP(11);
            f32x4 acc = {0.f, 0.f, 0.f, 0.f};
#pragma unroll
            for (int t = 0; t < 8; ++t) acc = mfma16(sel0, v[t], acc);
            const float o0 = acc.x + dpp<0x128>(acc.x) + bflo(xr.x), o1 = acc.y + dpp<0x128>(acc.y) + bfhi(xr.x);
            const float o2 = acc.z + dpp<0x128>(acc.z) + bflo(xr.y), o3 = acc.w + dpp<0x128>(acc.w) + bfhi(xr.y);
            if (g < 2 && p16 < 8) *(GAS v2u*)(out + oi) = (v2u){pk2(o0, o1), pk2(o2, o3)};
            if (r == 0 && l == 0) gst32(rs, W_EPOCH, tag);
            STAMP(15);
        }
        STAMP_OUT();
        return;
    }

    const bool isO = ix < 8;
    const int fs = xc * 7 + (ix - 8);
    {
        constexpr unsigned L_WUK = 131072, L_QL = 126976, L_QA = L_QL + 2048;
        if (w >= 4) {
            const v4u pz = {POISON, POISON, POISON, POISON};
            const unsigned t = threadIdx.x - 256;
            gst128(rs, w == 4 ? W_CTXP + (unsigned)(hh * 4 + qd) * 1024u + l * 16 : W_BYTES, pz);
            gst128(rs, isO ? (t < 32 ? W_HP + (unsigned)(xc * 4096 + ix * 512) + t * 16 : W_BYTES) : (ix < 15 ? W_OUTP + (unsigned)fs * 4096u + t * 16 : W_BYTES), pz);
        } else {
            VMWAIT(0);
        }
        GET_TAG();
        STAMP(1);
        BAR();
        v4u af[4];
        {
            const unsigned a = L_WUK + (16 * w + p16) * 256 + (((4 * g) ^ (((p16 & 3) << 2) | (p16 >> 2))) * 16);
#pragma unroll
            for (int s = 0; s < 4; ++s) af[s] = lds128(a ^ (s * 16));
        }
        STAMP(2);
        if (w < 4) {
            __builtin_amdgcn_s_sleep(HOP1_SLEEP);
            const unsigned off = W_QP + (unsigned)(((hh * 4 + g) * 8 + 2 * w) * 256 + p16 * 16);
            v4u v[2];
            STAMP(3);
            _Pragma("unroll 1") for (int it = 0; it < SPIN_CAP; ++it) {
                CFENCE();
                v[0] = gld128(rs, off); v[1] = gld128(rs, off + 256);
                const unsigned d0 = ((v[0].x & 255u) | ((v[0].y & 255u) << 8) | ((v[0].z & 255u) << 16) | (v[0].w << 24)) ^ tag;
                const unsigned d1 = ((v[1].x & 255u) | ((v[1].y & 255u) << 8) | ((v[1].z & 255u) << 16) | (v[1].w << 24)) ^ tag;
                if (it == 0) STAMP(4);
                if (__all((d0 | d1) == 0u)) break;
            }
            CFENCE();
            STAMP(5);
            float q[4];
#pragma unroll
            for (int j = 0; j < 4; ++j) {
                auto r = __builtin_amdgcn_permlane32_swap(v[0][j], v[1][j], false, false);
                q[j] = xsum16(__uint_as_float(r[0]) + __uint_as_float(r[1]));
            }
            if ((g & 1) == 0) sts64(L_QL + (p16 >> 2) * 272 + w * 64 + (g >> 1) * 32 + (p16 & 3) * 8, (v2u){pk2(q[0], q[1]), pk2(q[2], q[3])});
        }
        BAR();
        STAMP(14);
        {
            v4u bf[4];
#pragma unroll
            for (int s = 0; s < 4; ++s) bf[s] = lds128(L_QL + (l & 3) * 272 + g * 64 + s * 16);
            SB();
            f32x4 acc = {0.f, 0.f, 0.f, 0.f};
#pragma unroll
            for (int s = 0; s < 4; ++s) acc = mfma16(af[s], bf[s], acc);
            const float sc = 0.08838834764831845f * 1.4426950408889634f;
            if (p16 < 4) sts64(L_QA + p16 * 256 + w * 32 + g * 8, (v2u){pk2(acc.x * sc, acc.y * sc), pk2(acc.z * sc, acc.w * sc)});
        }
        VMWAIT(0);
        STAMP(13);
        BAR();
        if (threadIdx.x < 96) {
            const unsigned t = threadIdx.x, b = t / 24, r = t - b * 24;
            const unsigned a = L_QA + b * 256 + (r & 7) * 32 + (r >> 3) * 12;
            const unsigned d0 = __float_as_uint(lds32(a)), d1 = __float_as_uint(lds32(a + 4)), d2 = __float_as_uint(lds32(a + 8));
            gst128(rs, W_QABS + (unsigned)(((b * 32 + hh) * 4 + qd) * 384) + r * 16, (v4u){d0, (d2 & 0xFFFFu) | (tag << 16), d1, (d2 >> 16) | (tag & 0xFFFF0000u)});
        }
        STAMP(6);
    }
    if (ix == 15) { STAMP_OUT(); return; }
    if (isO) __builtin_amdgcn_s_sleep(TAIL_SLEEP); else __builtin_amdgcn_s_sleep(FFN_SLEEP);
    constexpr unsigned L_W = 0, L_WD = 65536, L_ACT = 98304, L_GR = L_ACT + 8192, L_ST = L_GR + 2048;
    {
        const unsigned ch = (unsigned)((l & 7) ^ (((g & 1) | ((w & 1) << 1)) << 1));
        if (isO) {
            dma_loop(mksrd(wo, 4194304), (unsigned)(l >> 3) * 1024u + ch * 16, (unsigned)(512 * xc + 8 * w) * 1024u + ix * 128, 65536, L_W + w * 1024, 8192, 8);
        } else {
            const unsigned char* src = (const unsigned char*)(ch < 4 ? wg_ : wu_) + (size_t)(8 * w + (l >> 3)) * 3584 + fs * 64 + (ch & 3) * 16;
            _Pragma("unroll 1") for (int t = 0; t < 8; ++t) { dmag(src, L_W + (8 * t + w) * 1024); src += 64 * 3584; }
            const v4u srd = mksrd(wd, 1835008);
            const unsigned sw = (unsigned)(((2 * w) & 3) << 2 | (w >> 1));
            dma_loop(srd, (l ^ sw) * 16, (unsigned)(32 * fs + 2 * w) * 1024u, 16384, L_WD + 2 * w * 1024, 16384, 2);
            dma_loop(srd, (l ^ sw ^ 4) * 16, (unsigned)(32 * fs + 2 * w + 1) * 1024u, 16384, L_WD + (2 * w + 1) * 1024, 16384, 2);
        }
        VMWAIT(0);
    }
    STAMP(7);
    BAR();
    v4u wf[16];
    {
        const unsigned a0 = L_W + (8 * g + trq) * 128 + (((2 * (w & 3) + (trp >> 1)) ^ ((((trq >> 1) & 1) | ((g & 1) << 1)) << 1)) * 16) + 8 * (trp & 1);
#pragma unroll
        for (int s = 0; s < 16; ++s) wf[s] = ldstr2(a0 + s * 4096, a0 + s * 4096 + 512);
    }
    const v4u one = {0x3F803F80u, 0x3F803F80u, 0x3F803F80u, 0x3F803F80u};
    const v4u sel0 = selmask(l, 0) & one, sel1 = selmask(l, 8) & one;
    const int pb = (p16 >> 1) & 3, pk = 32 * (g >> 1) + 16 * (p16 >> 3) + 8 * (p16 & 1) + 4 * (g & 1);
    const unsigned acta = L_ACT + pb * 1088 + (w * 64 + pk) * 2;
    if (isO) {
        if (w == 7) { wait_flag<1>(rs, W_HINT + (unsigned)(wg - 128) * 128u + (l & 1) * 4, tag); __builtin_amdgcn_s_sleep(H4_SLEEP); }
        BAR();
        STAMP(8);
        v4u v[2];
        gather<2>(rs, W_CTXP + (unsigned)(((4 * xc + (w >> 1)) * 4 + g) * 1024 + (w & 1) * 512 + p16 * 16), 256, v);
        STAMP(9);
        if (w == 7 && l == 0) gst64(rs, W_HINT + wg * 128, (v2u){tag, tag});
        f32x4 acc = {0.f, 0.f, 0.f, 0.f};
        acc = mfma16(sel0, v[0], acc); acc = mfma16(sel1, v[1], acc);
        sts64(acta, (v2u){pk2(acc.x, acc.y), pk2(acc.z, acc.w)});
        BAR();
        if (w < 4) {
            v4u bf[16];
#pragma unroll
            for (int s = 0; s < 16; ++s) bf[s] = lds128(L_ACT + (l & 3) * 1088 + g * 16 + s * 64);
            SB();
            f32x4 d = {0.f, 0.f, 0.f, 0.f};
#pragma unroll
            for (int s = 0; s < 16; ++s) d = mfma16(wf[s], bf[s], d);
            if (p16 < 4) gst64(rs, W_HP + (unsigned)(xc * 4096 + (ix * 4 + w) * 128 + p16 * 32 + g * 8), (v2u){pk2(d.x, d.y), pk2(d.z, d.w)});
        }
        STAMP(10);
    } else {
        v4u df[4];
        {
            const unsigned r0 = 8 * g + trq;
#pragma unroll
            for (int k = 0; k < 4; ++k) {
                const unsigned ch = 8 * w + 4 * (k >> 1) + trp;
                const unsigned a0 = L_WD + r0 * 1024 + ((ch ^ ((trq << 2) | ((2 * g) & 3))) * 16) + 8 * (k & 1);
                df[k] = ldstr2(a0, (a0 ^ 16) + 4096);
            }
        }
        const v2u xr = *(const GAS v2u*)(x + pb * 512 + w * 64 + pk);
        if (w == 0) sts128(L_ACT + 4 * 1088 + l * 16, one);
        if (w == 7) { wait_flag<1>(rs, W_HINT + (unsigned)(wg - 64) * 128u + (l & 1) * 4, tag); __builtin_amdgcn_s_sleep(H5_SLEEP); }
        BAR();
        STAMP(8);
        v4u v[4];
        {
            const unsigned off = W_HP + (unsigned)(g * 4096 + w * 512 + p16 * 16);
            _Pragma("unroll 1") for (int it = 0; it < SPIN_CAP; ++it) {
                CFENCE();
                v[0] = gld128(rs, off); v[1] = gld128(rs, off, 16384); v[2] = gld128(rs, off + 256); v[3] = gld128(rs, off + 256, 16384);
                const unsigned m = umax4(v[3], umax4(v[2], umax4(v[1], umax4(v[0], 0u))));
                if (__all(m != POISON)) break;
            }
            CFENCE();
        }
        STAMP(9);
        f32x4 acc = {bflo(xr.x), bfhi(xr.x), bflo(xr.y), bfhi(xr.y)};
        acc = mfma16(sel0, v[0], acc); acc = mfma16(sel0, v[1], acc); acc = mfma16(sel1, v[2], acc); acc = mfma16(sel1, v[3], acc);
        sts64(acta, (v2u){pk2(acc.x, acc.y), pk2(acc.z, acc.w)});
        BAR();
        if (w < 4) {
            const int rb = p16 < 4 ? p16 : 4;
            v4u bf[16];
#pragma unroll
            for (int s = 0; s < 16; ++s) bf[s] = lds128(L_ACT + rb * 1088 + g * 16 + s * 64);
            SB();
            f32x4 d = {0.f, 0.f, 0.f, 0.f};
#pragma unroll
            for (int s = 0; s < 16; ++s) d = mfma16(wf[s], bf[s], d);
            if (p16 < 5) sts128(L_GR + p16 * 272 + w * 64 + g * 16, __builtin_bit_cast(v4u, d));
        } else {
            const v4u hv = lds128(L_ACT + (w - 4) * 1088 + l * 16);
            float t[8] = {bflo(hv.x), bfhi(hv.x), bflo(hv.y), bfhi(hv.y), bflo(hv.z), bfhi(hv.z), bflo(hv.w), bfhi(hv.w)};
            const float mean = wsum(((t[0] + t[1]) + (t[2] + t[3])) + ((t[4] + t[5]) + (t[6] + t[7]))) * (1.f / 512.f);
            float ss = 0.f;
#pragma unroll
            for (int j = 0; j < 8; ++j) { t[j] -= mean; ss += t[j] * t[j]; }
            const float rstd = rsqrtf(wsum(ss) * (1.f / 512.f) + 1e-5f);
            if (l == 0) sts64(L_ST + (w - 4) * 8, (v2u){__float_as_uint(mean), __float_as_uint(rstd)});
            if (w == 7 && l == 0) gst64(rs, W_HINT + wg * 128, (v2u){tag, tag});
        }
        BAR();
        STAMP(10);
        {
            const unsigned a = L_GR + (l & 3) * 272 + g * 32, c = L_GR + 4 * 272 + g * 32;
            const f32x4 g0 = __builtin_bit_cast(f32x4, lds128(a)), g1 = __builtin_bit_cast(f32x4, lds128(a + 16));
            const f32x4 u0 = __builtin_bit_cast(f32x4, lds128(a + 128)), u1 = __builtin_bit_cast(f32x4, lds128(a + 144));
            const f32x4 cg0 = __builtin_bit_cast(f32x4, lds128(c)), cg1 = __builtin_bit_cast(f32x4, lds128(c + 16));
            const f32x4 cu0 = __builtin_bit_cast(f32x4, lds128(c + 128)), cu1 = __builtin_bit_cast(f32x4, lds128(c + 144));
            const float mean = lds32(L_ST + (l & 3) * 8), rstd = lds32(L_ST + (l & 3) * 8 + 4);
            SB();
            const float r2 = rstd * rstd;
            float a8[8];
#pragma unroll
            for (int j = 0; j < 4; ++j) {
                a8[j] = fmaxf(g0[j] - mean * cg0[j], 0.f) * (u0[j] - mean * cu0[j]) * r2;
                a8[4 + j] = fmaxf(g1[j] - mean * cg1[j], 0.f) * (u1[j] - mean * cu1[j]) * r2;
            }
            const v4u af = {pk2(a8[0], a8[1]), pk2(a8[2], a8[3]), pk2(a8[4], a8[5]), pk2(a8[6], a8[7])};
            const f32x4 z = {0.f, 0.f, 0.f, 0.f};
            const f32x4 d0 = mfma16(df[0], af, z), d1 = mfma16(df[1], af, z), d2 = mfma16(df[2], af, z), d3 = mfma16(df[3], af, z);
            if (p16 < 4) {
                const unsigned o = W_OUTP + (unsigned)(fs * 4096 + w * 512 + (g >> 1) * 128 + p16 * 32 + (g & 1) * 16);
                gst128(rs, o, (v4u){pk2(d0.x, d0.y), pk2(d0.z, d0.w), pk2(d1.x, d1.y), pk2(d1.z, d1.w)});
                gst128(rs, o + 256, (v4u){pk2(d2.x, d2.y), pk2(d2.z, d2.w), pk2(d3.x, d3.y), pk2(d3.z, d3.w)});
            }
        }
        STAMP(11);
    }
    STAMP_OUT();
}

__global__ void __launch_bounds__(64) k_spin(const unsigned* st) {
    unsigned t0 = st[0]; int mn = 0, mx = 0;
    for (int i = threadIdx.x; i < 2048; i += 64) { mn = min(mn, (int)(st[i * 16] - t0)); if (i >= 0 * 8 && i < 0 * 8 && st[i * 16 + 0]) mx = max(mx, (int)(st[i * 16 + 0] - t0)); }
    for (int o = 32; o; o >>= 1) { mn = min(mn, __shfl_xor(mn, o)); mx = max(mx, __shfl_xor(mx, o)); }
    int m = 0;
    if (m > 5000) m = 5000;
    unsigned long long a = __builtin_amdgcn_s_memrealtime();
    while (__builtin_amdgcn_s_memrealtime() < a + (unsigned long long)m * 20) __builtin_amdgcn_s_sleep(2);
}
extern "C" void kernel_launch(void* const* d_in, const int* in_sizes, int n_in, void* d_out, int out_size, void* d_ws, size_t ws_size,
                              hipStream_t stream) {
    (void)in_sizes; (void)n_in; (void)out_size;
    unsigned char* arena = (unsigned char*)d_ws + ((ws_size - W_BYTES) & ~(size_t)4095);
    (void)hipFuncSetAttribute((const void*)fused_decode, hipFuncAttributeMaxDynamicSharedMemorySize, LDS_BYTES);
    fused_decode<<<256, 512, LDS_BYTES, stream>>>((const u16*)d_in[2], (const u16*)d_in[3], (const u16*)d_in[1], (const u16*)d_in[0], arena, (const u16*)d_in[4],
                                                  (const u16*)d_in[5], (const u16*)d_in[7], (const u16*)d_in[8], (const u16*)d_in[9], (u16*)d_out);
    k_spin<<<1, 64, 0, stream>>>((const unsigned*)(arena + W_STAMP));
}
```

```cpp
#define STAMPS
#define UK_SLEEP "6"
#define KV_SLEEP 14
#include <hip/hip_runtime.h>
#include <stdint.h>

#define GAS __attribute__((address_space(1)))
#define LAS __attribute__((address_space(3)))
typedef unsigned short u16;
typedef unsigned v4u __attribute__((ext_vector_type(4)));
typedef unsigned v2u __attribute__((ext_vector_type(2)));
typedef float f32x4 __attribute__((ext_vector_type(4)));
typedef float f32x2 __attribute__((ext_vector_type(2)));
typedef float f32x16 __attribute__((ext_vector_type(16)));
typedef short s16x4 __attribute__((ext_vector_type(4)));
typedef __bf16 bf16x8 __attribute__((ext_vector_type(8)));
typedef __bf16 bf16x2 __attribute__((ext_vector_type(2)));

#define KNOB(n, v)
#define NT_POLICY " nt"
#define GU_POLICY ""
#define HOP1_SLEEP 6
#define STAT_SLEEP 16
#define Q_SLEEP 0
#define TAIL_SLEEP 60
#define FFN_SLEEP 40
#define PART_SLEEP 10
#define H4_SLEEP 10
#define H5_SLEEP 12
#define H6_SLEEP 10
#define POISON_PART 3
#define CODE_BYTES "17600"

constexpr unsigned W_EPOCH = 0;
constexpr unsigned W_HINT = 4096;
constexpr unsigned W_QP = W_HINT + 32768;
constexpr unsigned W_QABS = W_QP + 262144;
constexpr unsigned W_STAT = W_QABS + 196608;
constexpr unsigned W_CTXP = W_STAT + 65536;
constexpr unsigned W_OUTP = W_CTXP + 131072;
constexpr unsigned W_HP = W_OUTP + 56 * 4096;
constexpr unsigned W_PART = 1u << 20;
constexpr unsigned W_STAMP = W_PART + (4u << 20);
constexpr unsigned W_BYTES = 6u << 20;
static_assert(W_OUTP + 64 * 4096 <= W_PART, "arena");
constexpr int LDS_BYTES = 163840;
constexpr unsigned POISON = 0xFFFFFFFFu;
constexpr int SPIN_CAP = 1 << 16;

__device__ __forceinline__ unsigned pk2(float a, float b) { f32x2 v = {a, b}; return __builtin_bit_cast(unsigned, __builtin_convertvector(v, bf16x2)); }
__device__ __forceinline__ float bflo(unsigned u) { return __uint_as_float(u << 16); }
__device__ __forceinline__ float bfhi(unsigned u) { return __uint_as_float(u & 0xFFFF0000u); }
__device__ __forceinline__ f32x4 mfma16(v4u a, v4u b, f32x4 c) { return __builtin_amdgcn_mfma_f32_16x16x32_bf16(__builtin_bit_cast(bf16x8, a), __builtin_bit_cast(bf16x8, b), c, 0, 0, 0); }
__device__ __forceinline__ f32x16 mfma32(v4u a, v4u b, f32x16 c) { return __builtin_amdgcn_mfma_f32_32x32x16_bf16(__builtin_bit_cast(bf16x8, a), __builtin_bit_cast(bf16x8, b), c, 0, 0, 0); }
__device__ __forceinline__ v4u lds128(unsigned a) { return *(LAS v4u*)(uintptr_t)a; }
__device__ __forceinline__ v2u ldstr(unsigned a) { return __builtin_bit_cast(v2u, __builtin_amdgcn_ds_read_tr16_b64_v4i16((LAS s16x4*)(uintptr_t)a)); }
__device__ __forceinline__ v4u ldstr2(unsigned a0, unsigned a1) { v2u x = ldstr(a0), y = ldstr(a1); return (v4u){x.x, x.y, y.x, y.y}; }
__device__ __forceinline__ void sts128(unsigned a, v4u v) { *(LAS v4u*)(uintptr_t)a = v; }
__device__ __forceinline__ void sts64(unsigned a, v2u v) { *(LAS v2u*)(uintptr_t)a = v; }
__device__ __forceinline__ void sts32(unsigned a, float v) { *(LAS float*)(uintptr_t)a = v; }
__device__ __forceinline__ float lds32(unsigned a) { return *(LAS float*)(uintptr_t)a; }
#define BAR() asm volatile("s_waitcnt lgkmcnt(0)\n\ts_barrier" ::: "memory")
#define VMWAIT(n) asm volatile("s_waitcnt vmcnt(" #n ")" ::: "memory")
#define CFENCE() asm volatile("" ::: "memory")
typedef __amdgpu_buffer_rsrc_t rsrc_t;
__device__ __forceinline__ v4u gld128(rsrc_t r, unsigned off, unsigned so = 0) { return __builtin_amdgcn_raw_buffer_load_b128(r, off, so, 16); }
__device__ __forceinline__ v2u gld64(rsrc_t r, unsigned off) { return __builtin_amdgcn_raw_buffer_load_b64(r, off, 0, 16); }
__device__ __forceinline__ void gst128(rsrc_t r, unsigned off, v4u v, unsigned so = 0) { __builtin_amdgcn_raw_buffer_store_b128(v, r, off, so, 16); }
__device__ __forceinline__ void gst64(rsrc_t r, unsigned off, v2u v) { __builtin_amdgcn_raw_buffer_store_b64(v, r, off, 0, 16); }
__device__ __forceinline__ void gst32(rsrc_t r, unsigned off, unsigned v) { __builtin_amdgcn_raw_buffer_store_b32(v, r, off, 0, 16); }
__device__ __forceinline__ v4u mksrd(const void* p, unsigned bytes) { const uint64_t a = (uint64_t)p; return (v4u){(unsigned)a, (unsigned)(a >> 32) & 0xFFFFu, bytes, 0x00020000u}; }
__device__ __forceinline__ void dma(v4u srd, unsigned voff, unsigned soff, unsigned dst) {
    asm volatile("s_mov_b32 m0, %3\n\ts_nop 0\n\tbuffer_load_dwordx4 %0, %1, %2 offen" NT_POLICY " lds" :: "v"(voff), "s"(srd), "s"(soff), "s"(dst) : "memory");
}
__device__ __forceinline__ void dmag(const void* p, unsigned dst) {
    asm volatile("s_mov_b32 m0, %1\n\ts_nop 0\n\tglobal_load_lds_dwordx4 %0, off" GU_POLICY :: "v"(p), "s"(dst) : "memory");
}
__device__ __forceinline__ void dma_loop(v4u srd, unsigned voff, unsigned soff, unsigned sstep, unsigned dst, unsigned dstep, unsigned n) {
    asm volatile("s_mov_b32 m0, %4\n\ts_nop 0\n"
                 ".Ldl%=:\n\t"
                 "buffer_load_dwordx4 %2, %3, %0 offen" NT_POLICY " lds\n\t"
                 "s_add_u32 m0, m0, %6\n\ts_add_u32 %0, %0, %5\n\ts_sub_u32 %1, %1, 1\n\ts_cmp_lg_u32 %1, 0\n\ts_cbranch_scc1 .Ldl%="
                 : "+s"(soff), "+s"(n) : "v"(voff), "s"(srd), "s"(dst), "s"(sstep), "s"(dstep) : "scc", "memory");
}
__device__ __forceinline__ unsigned umax4(v4u v, unsigned m) { return max(max(max(v.x, v.y), max(v.z, v.w)), m); }
__device__ __forceinline__ float xsum16(float v) { auto r = __builtin_amdgcn_permlane16_swap(__float_as_uint(v), __float_as_uint(v), false, false); return __uint_as_float(r[0]) + __uint_as_float(r[1]); }
__device__ __forceinline__ float xsum32(float v) { auto r = __builtin_amdgcn_permlane32_swap(__float_as_uint(v), __float_as_uint(v), false, false); return __uint_as_float(r[0]) + __uint_as_float(r[1]); }
__device__ __forceinline__ float xmax16(float v) { auto r = __builtin_amdgcn_permlane16_swap(__float_as_uint(v), __float_as_uint(v), false, false); return fmaxf(__uint_as_float(r[0]), __uint_as_float(r[1])); }
__device__ __forceinline__ float xmax32(float v) { auto r = __builtin_amdgcn_permlane32_swap(__float_as_uint(v), __float_as_uint(v), false, false); return fmaxf(__uint_as_float(r[0]), __uint_as_float(r[1])); }
template <int CTRL> __device__ __forceinline__ float dpp(float v) { return __int_as_float(__builtin_amdgcn_update_dpp(0, __float_as_int(v), CTRL, 0xf, 0xf, true)); }
__device__ __forceinline__ float rsum16(float v) { v += dpp<0xB1>(v); v += dpp<0x4E>(v); v += dpp<0x141>(v); v += dpp<0x140>(v); return v; }
__device__ __forceinline__ float rmax16(float v) { v = fmaxf(v, dpp<0xB1>(v)); v = fmaxf(v, dpp<0x4E>(v)); v = fmaxf(v, dpp<0x141>(v)); v = fmaxf(v, dpp<0x140>(v)); return v; }
__device__ __forceinline__ float wsum(float v) { return xsum32(xsum16(rsum16(v))); }

#define STAMP(k) do { __builtin_amdgcn_sched_barrier(0); unsigned long long t_; asm volatile("s_memrealtime %0\n\ts_waitcnt lgkmcnt(0)" : "=s"(t_) :: "memory"); \
    stv[k] = (unsigned)t_; __builtin_amdgcn_sched_barrier(0); } while (0)
#define STAMP_OUT() do { if (l == 0) { GAS unsigned* sp_ = (GAS unsigned*)(ws + W_STAMP) + (wg * 8 + w) * 16; _Pragma("unroll") for (int k_ = 0; k_ < 16; ++k_) sp_[k_] = stv[k_]; } } while (0)

__device__ __forceinline__ v4u selmask(int l, int r0) {
    const int j = (l & 15) - r0;
    const unsigned m = (j & 1) ? 0xFFFF0000u : 0x0000FFFFu;
    return (v4u){(j >> 1) == 0 ? m : 0u, (j >> 1) == 1 ? m : 0u, (j >> 1) == 2 ? m : 0u, (j >> 1) == 3 ? m : 0u};
}

__device__ __forceinline__ unsigned gld32(rsrc_t r, unsigned off) { return __builtin_amdgcn_raw_buffer_load_b32(r, off, 0, 16); }
template <int SLEEP> __device__ __forceinline__ void wait_flag(rsrc_t rs, unsigned off, unsigned tag) {
    _Pragma("unroll 1") for (int it = 0; it < SPIN_CAP; ++it) {
        CFENCE();
        const unsigned v = gld32(rs, off);
        if (__all(v == tag)) break;
        __builtin_amdgcn_s_sleep(SLEEP);
    }
    CFENCE();
}
#define SB() __builtin_amdgcn_sched_barrier(0)
template <int N> __device__ __forceinline__ void gather(rsrc_t rs, unsigned off, unsigned stride, v4u (&v)[N]) {
    _Pragma("unroll 1") for (int it = 0; it < SPIN_CAP; ++it) {
        CFENCE();
        unsigned m = 0;
#pragma unroll
        for (int k = 0; k < N; ++k) v[k] = gld128(rs, off, k * stride);
#pragma unroll
        for (int k = 0; k < N; ++k) m = umax4(v[k], m);
        if (__all(m != POISON)) break;
    }
    CFENCE();
}

__global__ void __launch_bounds__(512, 2)
fused_decode(const u16* __restrict__ wq, const u16* __restrict__ wuk, const u16* __restrict__ ckv_arg, const u16* __restrict__ x,
             unsigned char* __restrict__ ws_arg, const u16* __restrict__ wuv, const u16* __restrict__ wo, const u16* __restrict__ wg_,
             const u16* __restrict__ wu_, const u16* __restrict__ wd, u16* __restrict__ out) {
    extern __shared__ __attribute__((aligned(16))) unsigned char lds_[];
    unsigned stv[16];
#pragma unroll
    for (int k = 0; k < 16; ++k) stv[k] = 0;
    STAMP(0);
    v4u xv;
    unsigned rawtag;
    unsigned char* ws;
    const u16* ckv;
    {
        unsigned t0, t1, t2, t3, pfs;
#define PDMA(V) "buffer_load_dwordx4 " V ", s[52:55], s58 offen" NT_POLICY " lds\n\t"
#define PSTEP "s_add_u32 m0, m0, 0x400\n\ts_add_u32 s58, s58, 0x1000\n\t"
        asm volatile(
            "s_load_dwordx8 s[40:47], %[ka], 0x0\n\t"
            "s_load_dwordx2 %[ws], %[ka], 0x20\n\t"
            "v_readfirstlane_b32 s50, %[tid]\n\t"
            "s_bfe_u32 s51, %[wg], 0x40003\n\t"
            "s_lshr_b32 s50, s50, 6\n\t"
            "s_cmp_lt_u32 s50, 4\n\t"
            "s_cbranch_scc1 .Ldm%=\n\t"
            "s_cmp_lt_u32 %[wg], 0x80\n\t"
            "s_cbranch_scc1 .Lxl%=\n\t"
            "s_getpc_b64 s[56:57]\n\t"
            "s_lshl_b32 s60, s51, 2\n\t"
            "s_add_u32 s60, s60, s50\n\t"
            "s_mul_i32 s60, s60, 320\n\t"
            "s_sub_u32 s60, s60, 1280\n\t"
            "s_min_u32 s60, s60, " CODE_BYTES "\n\t"
            "s_add_u32 s56, s56, s60\n\t"
            "s_addc_u32 s57, s57, 0\n\t"
            "s_lshl_b32 s58, %[wg], 15\n\t"
            "s_add_u32 s58, s58, 0xffcff000\n\t"
            "v_lshl_add_u32 %[t0], %[tid], 4, s58\n\t"
            "v_mov_b32 %[t1], -1\n\t"
            "s_mov_b32 s54, 0x600000\n\t"
            "s_mov_b32 s55, 0x20000\n\t"
            "s_mov_b32 s58, 0\n\t"
            "s_waitcnt lgkmcnt(0)\n\t"
            "s_mov_b64 s[52:53], %[ws]\n\t"
            "s_and_b32 s53, s53, 0xffff\n\t"
            "s_load_dword %[pf], s[56:57], 0x0\n\t"
            "s_load_dword %[pf], s[56:57], 0x40\n\t"
            "s_load_dword %[pf], s[56:57], 0x80\n\t"
            "s_load_dword %[pf], s[56:57], 0xc0\n\t"
            "s_load_dword %[pf], s[56:57], 0x100\n"
            ".Lpz%=:\n\t"
            "buffer_store_dwordx4 %[pz], %[t0], s[52:55], s58 offen sc1\n\t"
            "s_add_u32 s58, s58, 0x1000\n\t"
            "s_cmp_lg_u32 s58, 0x8000\n\t"
            "s_cbranch_scc1 .Lpz%=\n\t"
            "s_waitcnt lgkmcnt(0)\n\t"
            "s_load_dword %[tag], %[ws], 0x0\n\t"
            "s_branch .Lpe%=\n"
            ".Lxl%=:\n\t"
            "v_lshlrev_b32 %[t1], 4, %[tid]\n\t"
            "s_waitcnt lgkmcnt(0)\n\t"
            "global_load_dwordx4 %[xv], %[t1], s[46:47] offset:-4096\n\t"
            "s_load_dword %[tag], %[ws], 0x0\n\t"
            "s_branch .Lpe%=\n"
            ".Ldm%=:\n\t"
            "v_bfe_u32 %[t2], %[tid], 4, 2\n\t"
            "v_and_b32 %[t3], 15, %[tid]\n\t"
            "s_and_b32 s59, %[wg], 7\n\t"
            "s_lshl_b32 s62, s50, 15\n\t"
            "s_lshl_b32 s58, s59, 10\n\t"
            "s_lshr_b32 s60, s51, 2\n\t"
            "s_lshl_b32 s60, s60, 20\n\t"
            "s_and_b32 s61, s51, 3\n\t"
            "s_lshl_b32 s61, s61, 8\n\t"
            "s_add_u32 s58, s58, s62\n\t"
            "s_add_u32 s60, s60, s61\n\t"
            "s_add_u32 s58, s58, s60\n\t"
            "v_lshlrev_b32 %[t0], 2, %[t2]\n\t"
            "v_xor_b32 %[t1], s50, %[t3]\n\t"
            "v_xor_b32 %[t1], %[t0], %[t1]\n\t"
            "v_lshlrev_b32 %[t0], 13, %[t2]\n\t"
            "v_lshl_add_u32 %[t0], %[t1], 4, %[t0]\n\t"
            "s_lshl_b32 s60, s50, 10\n\t"
            "s_add_u32 m0, s60, 0x20000\n\t"
            "s_mov_b32 s54, 0x400000\n\t"
            "s_mov_b32 s55, 0x20000\n\t"
            "s_mov_b32 s61, 8\n\t"
            "s_cmp_lt_u32 %[wg], 0x80\n\t"
            "s_waitcnt lgkmcnt(0)\n\t"
            "s_cselect_b32 s52, s40, s42\n\t"
            "s_cselect_b32 s53, s41, s43\n\t"
            "s_and_b32 s53, s53, 0xffff\n\t"
            "s_mov_b64 %[kv], s[44:45]\n\t"
            "s_load_dword %[tag], %[ws], 0x0\n\t"
            "s_cmp_lt_u32 %[wg], 0x80\n\t"
            "s_cbranch_scc1 .Lf1%=\n\t"
            "s_sleep " UK_SLEEP "\n"
            ".Lf1%=:\n\t"
            PDMA("%[t0]")
            "s_add_u32 m0, m0, 0x1000\n\ts_add_u32 s58, s58, 0x20000\n\ts_sub_u32 s61, s61, 1\n\ts_cmp_lg_u32 s61, 0\n\ts_cbranch_scc1 .Lf1%=\n"
            ".Lpe%=:"
            : [xv] "=&v"(xv), [tag] "=&s"(rawtag), [ws] "=&s"(ws), [kv] "=&s"(ckv), [pf] "=&s"(pfs), [t0] "=&v"(t0), [t1] "=&v"(t1), [t2] "=&v"(t2), [t3] "=&v"(t3)
            : [ka] "s"(__builtin_amdgcn_kernarg_segment_ptr()), [tid] "v"(threadIdx.x), [wg] "s"(blockIdx.x), [pz] "v"((v4u){POISON, POISON, POISON, POISON})
            : "s40", "s41", "s42", "s43", "s44", "s45", "s46", "s47", "s50", "s51", "s52", "s53", "s54", "s55", "s56", "s57", "s58", "s59", "s60", "s61", "s62", "scc", "memory");
    }
    SB();
    const int wg = blockIdx.x, l = threadIdx.x & 63, w = __builtin_amdgcn_readfirstlane(threadIdx.x >> 6);
    const int xc = wg & 7, ix = (wg >> 3) & 15;
    const int g = l >> 4, p16 = l & 15;
    const int hh = 4 * xc + (ix & 3), qd = ix >> 2;
    const int ab = xc >> 1, arow = xc * 16 + ix;
    unsigned tag;
#define GET_TAG() asm volatile("s_waitcnt lgkmcnt(0)\n\ts_add_u32 %0, %1, 0x01010101" : "=s"(tag) : "s"(rawtag) : "scc", "memory")
    const rsrc_t rs = __builtin_amdgcn_make_buffer_rsrc(ws, 0, W_BYTES, 0x00020000);
    const unsigned trq = (unsigned)(p16 >> 2), trp = (unsigned)(l & 3);
    const unsigned tra = (8 * g + trq) * 256 + (((2 * w + (trp >> 1)) ^ ((trq << 2) | ((2 * g) & 3))) * 16) + 8 * (trp & 1);

    if (__builtin_expect(wg < 128, 1)) {
        constexpr unsigned L_Q = 131072, L_XN = 98304, L_MX = 0, L_LS = 512, L_P = 1024, L_CL = 12288, L_WUV = 32768;
        auto kvload = [&](unsigned cb, unsigned n) {
            const unsigned v0 = (unsigned)g * 1024u + (unsigned)((p16 ^ (2 * g)) * 16);
            unsigned so = (unsigned)(arow * 128 + 32 * w) * 1024u + cb * 256;
#define KDMA(V) "buffer_load_dwordx4 " V ", %6, %0 offen" NT_POLICY " lds\n\t"
#define KSTEP "s_add_u32 m0, m0, 0x400\n\ts_add_u32 %0, %0, 0x1000\n\t"
            asm volatile("s_mov_b32 m0, %7\n\ts_nop 0\n"
                         ".Lkv%=:\n\t"
                         KDMA("%2") KSTEP KDMA("%3") KSTEP KDMA("%4") KSTEP KDMA("%5") KSTEP KDMA("%2") KSTEP KDMA("%3") KSTEP KDMA("%4") KSTEP KDMA("%5")
                         "s_add_u32 m0, m0, 25600\n\ts_add_u32 %0, %0, 0xffff9100\n\ts_sub_u32 %1, %1, 1\n\ts_cmp_lg_u32 %1, 0\n\ts_cbranch_scc1 .Lkv%="
                         : "+s"(so), "+s"(n) : "v"(v0), "v"(v0 ^ 128u), "v"(v0 ^ 16u), "v"(v0 ^ 144u), "s"(mksrd(ckv, 16777216u)), "s"(cb * 32768 + w * 8192) : "scc", "memory");
        };
        if (w < 4) {
            asm volatile("s_barrier" ::: "memory");
            __builtin_amdgcn_s_sleep(KV_SLEEP);
            kvload(0, 1);
            GET_TAG();
            STAMP(1);
            VMWAIT(8);
        } else {
            asm volatile("s_barrier" ::: "memory");
            GET_TAG();
            STAMP(1);
            asm volatile("s_waitcnt vmcnt(0)" : "+v"(xv) :: "memory");
            float v[8] = {bflo(xv.x), bfhi(xv.x), bflo(xv.y), bfhi(xv.y), bflo(xv.z), bfhi(xv.z), bflo(xv.w), bfhi(xv.w)};
            const float mean = wsum(((v[0] + v[1]) + (v[2] + v[3])) + ((v[4] + v[5]) + (v[6] + v[7]))) * (1.f / 512.f);
            float ss = 0.f;
#pragma unroll
            for (int j = 0; j < 8; ++j) { v[j] -= mean; ss += v[j] * v[j]; }
            const float rstd = rsqrtf(wsum(ss) * (1.f / 512.f) + 1e-5f);
            if (g == qd) sts128(L_XN + (w - 4) * 320 + p16 * 16, (v4u){pk2(v[0] * rstd, v[1] * rstd), pk2(v[2] * rstd, v[3] * rstd), pk2(v[4] * rstd, v[5] * rstd), pk2(v[6] * rstd, v[7] * rstd)});
        }
        STAMP(12);
        BAR();
        {
            v4u af[4], bf[4];
#pragma unroll
            for (int s = 0; s < 4; ++s) { af[s] = ldstr2(L_Q + tra + s * 8192, L_Q + (tra ^ 16) + 1024 + s * 8192); bf[s] = lds128(L_XN + (l & 3) * 320 + g * 16 + s * 64); }
            SB();
            f32x4 acc = {0.f, 0.f, 0.f, 0.f};
#pragma unroll
            for (int s = 0; s < 4; ++s) acc = mfma16(af[s], bf[s], acc);
            v4u o;
            o.x = (__float_as_uint(acc.x) & ~255u) | (tag & 255u); o.y = (__float_as_uint(acc.y) & ~255u) | ((tag >> 8) & 255u);
            o.z = (__float_as_uint(acc.z) & ~255u) | ((tag >> 16) & 255u); o.w = (__float_as_uint(acc.w) & ~255u) | (tag >> 24);
            gst128(rs, p16 < 4 ? W_QP + (unsigned)(((hh * 4 + qd) * 8 + w) * 256 + p16 * 64 + g * 16) : W_BYTES, o);
        }
        STAMP(13);
        BAR();
        v4u uf[4];
        if (w < 4) {
            kvload(1, 3);
            STAMP(14);
            const int r = l & 31, hf = l >> 5;
            const unsigned fa = (unsigned)(r * 256 + ((hf ^ (((r & 7) << 1) | ((r >> 3) & 1))) * 16));
            v4u kf[32];
            VMWAIT(16);
#pragma unroll
            for (int s = 0; s < 16; ++s) kf[s] = lds128((fa ^ ((s & 7) * 32)) + (s >> 3) * 32768 + w * 8192);
            BAR();
            STAMP(2);
            VMWAIT(8);
#pragma unroll
            for (int s = 16; s < 24; ++s) kf[s] = lds128((fa ^ ((s & 7) * 32)) + (s >> 3) * 32768 + w * 8192);
            VMWAIT(0);
            STAMP(3);
#pragma unroll
            for (int s = 24; s < 32; ++s) kf[s] = lds128((fa ^ ((s & 7) * 32)) + (s >> 3) * 32768 + w * 8192);
            BAR();
            STAMP(4);
            f32x16 sa;
#pragma unroll
            for (int j = 0; j < 16; ++j) sa[j] = 0.f;
            {
                v4u qf[32];
#pragma unroll
                for (int s = 0; s < 12; ++s) qf[s] = lds128(L_Q + (fa ^ ((s & 7) * 32)) + (s >> 3) * 8192);
                SB();
#pragma unroll
                for (int s = 0; s < 32; ++s) {
                    sa = mfma32(kf[s], qf[s], sa);
                    if (s + 12 < 32) qf[s + 12] = lds128(L_Q + (fa ^ (((s + 12) & 7) * 32)) + ((s + 12) >> 3) * 8192);
                    SB();
                }
            }
            float m = sa[0];
#pragma unroll
            for (int j = 1; j < 16; ++j) m = fmaxf(m, sa[j]);
            m = xmax32(m);
            if (hf == 0) sts32(L_MX + r * 16 + w * 4, m);
            STAMP(5);
            BAR();
            const f32x4 mv = __builtin_bit_cast(f32x4, lds128(L_MX + r * 16));
            const float M = fmaxf(fmaxf(mv.x, mv.y), fmaxf(mv.z, mv.w));
            unsigned pw[8];
#pragma unroll
            for (int j = 0; j < 8; ++j) pw[j] = pk2(__builtin_amdgcn_exp2f(sa[2 * j] - M), __builtin_amdgcn_exp2f(sa[2 * j + 1] - M));
            const unsigned pa = L_P + (unsigned)((w * 2 + (r >> 4)) * 1024 + hf * 256 + (r & 15) * 16);
            sts128(pa, (v4u){pw[0], pw[1], pw[2], pw[3]});
            sts128(pa + 512, (v4u){pw[4], pw[5], pw[6], pw[7]});
            float ls = 0.f;
#pragma unroll
            for (int j = 0; j < 8; ++j) ls += bflo(pw[j]) + bfhi(pw[j]);
            ls = xsum32(ls);
            if (hf == 0) sts32(L_LS + r * 16 + w * 4, ls);
            BAR();
            STAMP(6);
            if (w == 0 && l < 32) {
                const f32x4 lv = __builtin_bit_cast(f32x4, lds128(L_LS + r * 16));
                gst128(rs, W_STAT + (unsigned)(arow * 512 + r * 16), (v4u){__float_as_uint(M), tag, __float_as_uint((lv.x + lv.y) + (lv.z + lv.w)), tag});
            }
            const unsigned sto = W_STAT + (unsigned)((w * 32 + r) * 512 + hh * 16), pvo = W_PART + (unsigned)(((hh * 4 + qd) * 128 + w * 32) * 256 + l * 16);
            const v4u sm = selmask(l, 0);
            BAR();
            STAMP(7);
            v4u st = gld128(rs, sto);
#pragma unroll
            for (int s = 0; s < 4; ++s) uf[s] = ldstr2(L_WUV + tra + s * 8192, L_WUV + (tra ^ 16) + 1024 + s * 8192);
            SB();
            __builtin_amdgcn_s_sleep(PART_SLEEP);
            SB();
            v4u pv[8];
#pragma unroll
            for (int k = 0; k < 8; ++k) pv[k] = gld128(rs, pvo, k * 1024);
            SB();
            if (!__all(st.y == tag && st.w == tag)) {
                _Pragma("unroll 1") for (int it = 0; it < SPIN_CAP; ++it) {
                    CFENCE();
                    st = gld128(rs, sto);
                    if (__all(st.y == tag && st.w == tag)) break;
                }
            }
            STAMP(8);
            const float ms = __uint_as_float(st.x);
            const float Mx = xmax16(rmax16(ms));
            const unsigned cb2 = pk2(__builtin_amdgcn_exp2f(ms - Mx), __builtin_amdgcn_exp2f(ms - Mx));
            const float rl = 1.f / xsum16(rsum16(bflo(cb2) * __uint_as_float(st.z)));
            v4u cf[8];
#pragma unroll
            for (int t = 0; t < 8; ++t) { const unsigned c = (unsigned)__builtin_amdgcn_ds_bpermute((4 * t + g) * 4, (int)cb2); cf[t] = sm & c; }
            SB();
            {
                unsigned mx = 0;
#pragma unroll
                for (int k = 0; k < 8; ++k) mx = umax4(pv[k], mx);
                STAMP(15);
                if (!__all(mx != POISON)) gather<8>(rs, pvo, 1024, pv);
            }
            STAMP(9);
            if (w == 0 && l == 0) gst64(rs, W_HINT + wg * 128, (v2u){tag, tag});
            f32x4 acc = {0.f, 0.f, 0.f, 0.f};
#pragma unroll
            for (int t = 0; t < 8; ++t) acc = mfma16(cf[t], pv[t], acc);
            if (g < 2) sts64(L_CL + w * 320 + p16 * 16 + g * 8, (v2u){pk2(acc.x * rl, acc.y * rl), pk2(acc.z * rl, acc.w * rl)});
        } else {
            const int ow = w - 4;
            const unsigned va = (unsigned)(ow * 32768 + (16 * (g >> 1) + 4 * (g & 1) + trq) * 256 + (((trp >> 1) ^ (8 * (g & 1) + 2 * trq)) * 16) + 8 * (trp & 1));
            const unsigned po = W_PART + (unsigned)(((16 * g + ow) * 128 + arow) * 256 + p16 * 16);
            v4u vf[4][8];
            auto vread = [&]() {
#pragma unroll
                for (int kg = 0; kg < 4; ++kg)
#pragma unroll
                    for (int ct = 0; ct < 8; ++ct) vf[kg][ct] = ldstr2((va ^ (ct * 32)) + kg * 8192, (va ^ (ct * 32 + 16)) + kg * 8192 + 2048);
            };
            {
                const unsigned off = W_QABS + (unsigned)((ab * 32 + 8 * ow) * 1536 + (l >> 3) * 384 + (l & 7) * 16);
                const unsigned qa = L_Q + (unsigned)(((l >> 3) & 3) * 8192 + (8 * ow + (l >> 5)) * 256 + (((2 * (l & 7)) ^ (((l >> 5) << 1) | (ow & 1))) * 16));
                BAR();
                STAMP(2);
                __builtin_amdgcn_s_sleep(Q_SLEEP);
                _Pragma("unroll 1") for (int it = 0;; ++it) {
                    CFENCE();
                    v4u qv[4][3];
#pragma unroll
                    for (int k = 0; k < 4; ++k)
#pragma unroll
                        for (int p = 0; p < 3; ++p) qv[k][p] = gld128(rs, off + p * 128, k * 3072);
                    SB();
                    if (ow < 2 && it == 0) vread();
                    unsigned d = 0;
#pragma unroll
                    for (int k = 0; k < 4; ++k) {
                        const unsigned a = (qa ^ (k * 64)) + k * 512;
                        sts128(a, (v4u){qv[k][0].x, qv[k][0].z, __builtin_amdgcn_perm(qv[k][0].w, qv[k][0].y, 0x05040100u), qv[k][1].x});
                        sts128(a ^ 16, (v4u){qv[k][1].z, __builtin_amdgcn_perm(qv[k][1].w, qv[k][1].y, 0x05040100u), qv[k][2].x, qv[k][2].z});
#pragma unroll
                        for (int p = 0; p < 3; ++p) d |= (qv[k][p].y ^ (tag << 16)) | (qv[k][p].w ^ tag);
                    }
                    if (it == 0) STAMP(3);
                    if (__all(d < 65536u) || it >= SPIN_CAP) break;
                }
                STAMP(4);
                BAR();
            }
            if (ow >= 2) {
                vread();
            } else {
                const v4u srd = mksrd(wuv, 4194304);
                const unsigned vo = (unsigned)g * (16u * 8192u) + (unsigned)((p16 ^ (g << 2) ^ ow) * 16), so = (unsigned)(128 * qd + 64 * ow) * 8192u + hh * 256;
                dma_loop(srd, vo, so, 16384, L_WUV + ow * 1024, 4096, 8);
                dma_loop(srd, vo ^ 32, so + 8192, 16384, L_WUV + (2 + ow) * 1024, 4096, 8);
            }
            STAMP(5);
            BAR();
            if (ow >= 2) VMWAIT(0);
            STAMP(14);
            BAR();
            STAMP(6);
            v4u pf[8];
#pragma unroll
            for (int k = 0; k < 8; ++k) pf[k] = lds128(L_P + ((k & 3) * 2 + (k >> 2)) * 1024 + l * 16);
            SB();
            f32x4 acc[2][8];
#pragma unroll
            for (int ct = 0; ct < 8; ++ct) { acc[0][ct] = (f32x4){0.f, 0.f, 0.f, 0.f}; acc[1][ct] = (f32x4){0.f, 0.f, 0.f, 0.f}; }
#pragma unroll
            for (int kg = 0; kg < 4; ++kg)
#pragma unroll
                for (int ct = 0; ct < 8; ++ct) acc[0][ct] = mfma16(pf[kg], vf[kg][ct], acc[0][ct]);
            SB();
#pragma unroll
            for (int kg = 0; kg < 4; ++kg) {
#pragma unroll
                for (int ct = 0; ct < 8; ++ct) acc[1][ct] = mfma16(pf[4 + kg], vf[kg][ct], acc[1][ct]);
                gst128(rs, po, (v4u){pk2(acc[0][0][kg], acc[0][1][kg]), pk2(acc[0][2][kg], acc[0][3][kg]), pk2(acc[0][4][kg], acc[0][5][kg]), pk2(acc[0][6][kg], acc[0][7][kg])}, kg * 131072);
                SB();
            }
            {
                v4u o[4];
#pragma unroll
                for (int j = 0; j < 4; ++j) o[j] = (v4u){pk2(acc[1][0][j], acc[1][1][j]), pk2(acc[1][2][j], acc[1][3][j]), pk2(acc[1][4][j], acc[1][5][j]), pk2(acc[1][6][j], acc[1][7][j])};
                asm volatile("" : "+v"(o[0]), "+v"(o[1]), "+v"(o[2]), "+v"(o[3]));
                STAMP(8);
#pragma unroll
                for (int j = 0; j < 4; ++j) gst128(rs, po, o[j], 2097152 + j * 131072);
            }
            STAMP(7);
            VMWAIT(8);
            BAR();
#pragma unroll
            for (int s = 0; s < 4; ++s) uf[s] = ldstr2(L_WUV + tra + s * 8192, L_WUV + (tra ^ 16) + 1024 + s * 8192);
        }
        {
            BAR();
            v4u bf[4];
#pragma unroll
            for (int s = 0; s < 4; ++s) bf[s] = lds128(L_CL + (l & 3) * 320 + g * 16 + s * 64);
            SB();
            f32x4 acc = {0.f, 0.f, 0.f, 0.f};
#pragma unroll
            for (int s = 0; s < 4; ++s) acc = mfma16(uf[s], bf[s], acc);
            if (p16 < 4) gst64(rs, W_CTXP + (unsigned)((hh * 4 + qd) * 1024 + w * 128 + p16 * 32 + g * 8), (v2u){pk2(acc.x, acc.y), pk2(acc.z, acc.w)});
        }
        STAMP(10);
        if (ix < 4 && w == 4) {
            const int r = xc * 4 + ix, oi = ((l >> 1) & 3) * 512 + r * 16 + (l & 1) * 8 + (g & 1) * 4;
            const v2u xr = *(const GAS v2u*)(x + oi);
            const v4u sel0 = selmask(l, 0) & 0x3F803F80u;
            wait_flag<1>(rs, W_HINT + (unsigned)(wg + 128 + 64) * 128u, tag);
            __builtin_amdgcn_s_sleep(H6_SLEEP);
            STAMP(9);
            v4u v[8];
            gather<8>(rs, W_OUTP + (unsigned)((l >> 3) * 4096 + r * 128 + (l & 7) * 16), 8 * 4096, v);
            STAMP(11);
            f32x4 acc = {0.f, 0.f, 0.f, 0.f};
#pragma unroll
            for (int t = 0; t < 8; ++t) acc = mfma16(sel0, v[t], acc);
            const float o0 = acc.x + dpp<0x128>(acc.x) + bflo(xr.x), o1 = acc.y + dpp<0x128>(acc.y) + bfhi(xr.x);
            const float o2 = acc.z + dpp<0x128>(acc.z) + bflo(xr.y), o3 = acc.w + dpp<0x128>(acc.w) + bfhi(xr.y);
            if (g < 2 && p16 < 8) *(GAS v2u*)(out + oi) = (v2u){pk2(o0, o1), pk2(o2, o3)};
            if (r == 0 && l == 0) gst32(rs, W_EPOCH, tag);
            STAMP(15);
        }
        STAMP_OUT();
        return;
    }

    const bool isO = ix < 8;
    const int fs = xc * 7 + (ix - 8);
    {
        constexpr unsigned L_WUK = 131072, L_QL = 126976, L_QA = L_QL + 2048;
        if (w >= 4) {
            const v4u pz = {POISON, POISON, POISON, POISON};
            const unsigned t = threadIdx.x - 256;
            gst128(rs, w == 4 ? W_CTXP + (unsigned)(hh * 4 + qd) * 1024u + l * 16 : W_BYTES, pz);
            gst128(rs, isO ? (t < 32 ? W_HP + (unsigned)(xc * 4096 + ix * 512) + t * 16 : W_BYTES) : (ix < 15 ? W_OUTP + (unsigned)fs * 4096u + t * 16 : W_BYTES), pz);
        } else {
            VMWAIT(0);
        }
        GET_TAG();
        STAMP(1);
        BAR();
        v4u af[4];
        {
            const unsigned a = L_WUK + (16 * w + p16) * 256 + (((4 * g) ^ (((p16 & 3) << 2) | (p16 >> 2))) * 16);
#pragma unroll
            for (int s = 0; s < 4; ++s) af[s] = lds128(a ^ (s * 16));
        }
        STAMP(2);
        if (w < 4) {
            __builtin_amdgcn_s_sleep(HOP1_SLEEP);
            const unsigned off = W_QP + (unsigned)(((hh * 4 + g) * 8 + 2 * w) * 256 + p16 * 16);
            v4u v[2];
            STAMP(3);
            _Pragma("unroll 1") for (int it = 0; it < SPIN_CAP; ++it) {
                CFENCE();
                v[0] = gld128(rs, off); v[1] = gld128(rs, off + 256);
                const unsigned d0 = ((v[0].x & 255u) | ((v[0].y & 255u) << 8) | ((v[0].z & 255u) << 16) | (v[0].w << 24)) ^ tag;
                const unsigned d1 = ((v[1].x & 255u) | ((v[1].y & 255u) << 8) | ((v[1].z & 255u) << 16) | (v[1].w << 24)) ^ tag;
                if (it == 0) STAMP(4);
                if (__all((d0 | d1) == 0u)) break;
            }
            CFENCE();
            STAMP(5);
            float q[4];
#pragma unroll
            for (int j = 0; j < 4; ++j) {
                auto r = __builtin_amdgcn_permlane32_swap(v[0][j], v[1][j], false, false);
                q[j] = xsum16(__uint_as_float(r[0]) + __uint_as_float(r[1]));
            }
            if ((g & 1) == 0) sts64(L_QL + (p16 >> 2) * 272 + w * 64 + (g >> 1) * 32 + (p16 & 3) * 8, (v2u){pk2(q[0], q[1]), pk2(q[2], q[3])});
        }
        BAR();
        STAMP(14);
        {
            v4u bf[4];
#pragma unroll
            for (int s = 0; s < 4; ++s) bf[s] = lds128(L_QL + (l & 3) * 272 + g * 64 + s * 16);
            SB();
            f32x4 acc = {0.f, 0.f, 0.f, 0.f};
#pragma unroll
            for (int s = 0; s < 4; ++s) acc = mfma16(af[s], bf[s], acc);
            const float sc = 0.08838834764831845f * 1.4426950408889634f;
            if (p16 < 4) sts64(L_QA + p16 * 256 + w * 32 + g * 8, (v2u){pk2(acc.x * sc, acc.y * sc), pk2(acc.z * sc, acc.w * sc)});
        }
        VMWAIT(0);
        STAMP(13);
        BAR();
        if (threadIdx.x < 96) {
            const unsigned t = threadIdx.x, b = t / 24, r = t - b * 24;
            const unsigned a = L_QA + b * 256 + (r & 7) * 32 + (r >> 3) * 12;
            const unsigned d0 = __float_as_uint(lds32(a)), d1 = __float_as_uint(lds32(a + 4)), d2 = __float_as_uint(lds32(a + 8));
            gst128(rs, W_QABS + (unsigned)(((b * 32 + hh) * 4 + qd) * 384) + r * 16, (v4u){d0, (d2 & 0xFFFFu) | (tag << 16), d1, (d2 >> 16) | (tag & 0xFFFF0000u)});
        }
        STAMP(6);
    }
    if (ix == 15) { STAMP_OUT(); return; }
    if (isO) __builtin_amdgcn_s_sleep(TAIL_SLEEP); else __builtin_amdgcn_s_sleep(FFN_SLEEP);
    constexpr unsigned L_W = 0, L_WD = 65536, L_ACT = 98304, L_GR = L_ACT + 8192, L_ST = L_GR + 2048;
    {
        const unsigned ch = (unsigned)((l & 7) ^ (((g & 1) | ((w & 1) << 1)) << 1));
        if (isO) {
            dma_loop(mksrd(wo, 4194304), (unsigned)(l >> 3) * 1024u + ch * 16, (unsigned)(512 * xc + 8 * w) * 1024u + ix * 128, 65536, L_W + w * 1024, 8192, 8);
        } else {
            const unsigned char* src = (const unsigned char*)(ch < 4 ? wg_ : wu_) + (size_t)(8 * w + (l >> 3)) * 3584 + fs * 64 + (ch & 3) * 16;
            _Pragma("unroll 1") for (int t = 0; t < 8; ++t) { dmag(src, L_W + (8 * t + w) * 1024); src += 64 * 3584; }
            const v4u srd = mksrd(wd, 1835008);
            const unsigned sw = (unsigned)(((2 * w) & 3) << 2 | (w >> 1));
            dma_loop(srd, (l ^ sw) * 16, (unsigned)(32 * fs + 2 * w) * 1024u, 16384, L_WD + 2 * w * 1024, 16384, 2);
            dma_loop(srd, (l ^ sw ^ 4) * 16, (unsigned)(32 * fs + 2 * w + 1) * 1024u, 16384, L_WD + (2 * w + 1) * 1024, 16384, 2);
        }
        VMWAIT(0);
    }
    STAMP(7);
    BAR();
    v4u wf[16];
    {
        const unsigned a0 = L_W + (8 * g + trq) * 128 + (((2 * (w & 3) + (trp >> 1)) ^ ((((trq >> 1) & 1) | ((g & 1) << 1)) << 1)) * 16) + 8 * (trp & 1);
#pragma unroll
        for (int s = 0; s < 16; ++s) wf[s] = ldstr2(a0 + s * 4096, a0 + s * 4096 + 512);
    }
    const v4u one = {0x3F803F80u, 0x3F803F80u, 0x3F803F80u, 0x3F803F80u};
    const v4u sel0 = selmask(l, 0) & one, sel1 = selmask(l, 8) & one;
    const int pb = (p16 >> 1) & 3, pk = 32 * (g >> 1) + 16 * (p16 >> 3) + 8 * (p16 & 1) + 4 * (g & 1);
    const unsigned acta = L_ACT + pb * 1088 + (w * 64 + pk) * 2;
    if (isO) {
        if (w == 7) { wait_flag<1>(rs, W_HINT + (unsigned)(wg - 128) * 128u + (l & 1) * 4, tag); __builtin_amdgcn_s_sleep(H4_SLEEP); }
        BAR();
        STAMP(8);
        v4u v[2];
        gather<2>(rs, W_CTXP + (unsigned)(((4 * xc + (w >> 1)) * 4 + g) * 1024 + (w & 1) * 512 + p16 * 16), 256, v);
        STAMP(9);
        if (w == 7 && l == 0) gst64(rs, W_HINT + wg * 128, (v2u){tag, tag});
        f32x4 acc = {0.f, 0.f, 0.f, 0.f};
        acc = mfma16(sel0, v[0], acc); acc = mfma16(sel1, v[1], acc);
        sts64(acta, (v2u){pk2(acc.x, acc.y), pk2(acc.z, acc.w)});
        BAR();
        if (w < 4) {
            v4u bf[16];
#pragma unroll
            for (int s = 0; s < 16; ++s) bf[s] = lds128(L_ACT + (l & 3) * 1088 + g * 16 + s * 64);
            SB();
            f32x4 d = {0.f, 0.f, 0.f, 0.f};
#pragma unroll
            for (int s = 0; s < 16; ++s) d = mfma16(wf[s], bf[s], d);
            if (p16 < 4) gst64(rs, W_HP + (unsigned)(xc * 4096 + (ix * 4 + w) * 128 + p16 * 32 + g * 8), (v2u){pk2(d.x, d.y), pk2(d.z, d.w)});
        }
        STAMP(10);
    } else {
        v4u df[4];
        {
            const unsigned r0 = 8 * g + trq;
#pragma unroll
            for (int k = 0; k < 4; ++k) {
                const unsigned ch = 8 * w + 4 * (k >> 1) + trp;
                const unsigned a0 = L_WD + r0 * 1024 + ((ch ^ ((trq << 2) | ((2 * g) & 3))) * 16) + 8 * (k & 1);
                df[k] = ldstr2(a0, (a0 ^ 16) + 4096);
            }
        }
        const v2u xr = *(const GAS v2u*)(x + pb * 512 + w * 64 + pk);
        if (w == 0) sts128(L_ACT + 4 * 1088 + l * 16, one);
        if (w == 7) { wait_flag<1>(rs, W_HINT + (unsigned)(wg - 64) * 128u + (l & 1) * 4, tag); __builtin_amdgcn_s_sleep(H5_SLEEP); }
        BAR();
        STAMP(8);
        v4u v[4];
        {
            const unsigned off = W_HP + (unsigned)(g * 4096 + w * 512 + p16 * 16);
            _Pragma("unroll 1") for (int it = 0; it < SPIN_CAP; ++it) {
                CFENCE();
                v[0] = gld128(rs, off); v[1] = gld128(rs, off, 16384); v[2] = gld128(rs, off + 256); v[3] = gld128(rs, off + 256, 16384);
                const unsigned m = umax4(v[3], umax4(v[2], umax4(v[1], umax4(v[0], 0u))));
                if (__all(m != POISON)) break;
            }
            CFENCE();
        }
        STAMP(9);
        f32x4 acc = {bflo(xr.x), bfhi(xr.x), bflo(xr.y), bfhi(xr.y)};
        acc = mfma16(sel0, v[0], acc); acc = mfma16(sel0, v[1], acc); acc = mfma16(sel1, v[2], acc); acc = mfma16(sel1, v[3], acc);
        sts64(acta, (v2u){pk2(acc.x, acc.y), pk2(acc.z, acc.w)});
        BAR();
        if (w < 4) {
            const int rb = p16 < 4 ? p16 : 4;
            v4u bf[16];
#pragma unroll
            for (int s = 0; s < 16; ++s) bf[s] = lds128(L_ACT + rb * 1088 + g * 16 + s * 64);
            SB();
            f32x4 d = {0.f, 0.f, 0.f, 0.f};
#pragma unroll
            for (int s = 0; s < 16; ++s) d = mfma16(wf[s], bf[s], d);
            if (p16 < 5) sts128(L_GR + p16 * 272 + w * 64 + g * 16, __builtin_bit_cast(v4u, d));
        } else {
            const v4u hv = lds128(L_ACT + (w - 4) * 1088 + l * 16);
            float t[8] = {bflo(hv.x), bfhi(hv.x), bflo(hv.y), bfhi(hv.y), bflo(hv.z), bfhi(hv.z), bflo(hv.w), bfhi(hv.w)};
            const float mean = wsum(((t[0] + t[1]) + (t[2] + t[3])) + ((t[4] + t[5]) + (t[6] + t[7]))) * (1.f / 512.f);
            float ss = 0.f;
#pragma unroll
            for (int j = 0; j < 8; ++j) { t[j] -= mean; ss += t[j] * t[j]; }
            const float rstd = rsqrtf(wsum(ss) * (1.f / 512.f) + 1e-5f);
            if (l == 0) sts64(L_ST + (w - 4) * 8, (v2u){__float_as_uint(mean), __float_as_uint(rstd)});
            if (w == 7 && l == 0) gst64(rs, W_HINT + wg * 128, (v2u){tag, tag});
        }
        BAR();
        STAMP(10);
        {
            const unsigned a = L_GR + (l & 3) * 272 + g * 32, c = L_GR + 4 * 272 + g * 32;
            const f32x4 g0 = __builtin_bit_cast(f32x4, lds128(a)), g1 = __builtin_bit_cast(f32x4, lds128(a + 16));
            const f32x4 u0 = __builtin_bit_cast(f32x4, lds128(a + 128)), u1 = __builtin_bit_cast(f32x4, lds128(a + 144));
            const f32x4 cg0 = __builtin_bit_cast(f32x4, lds128(c)), cg1 = __builtin_bit_cast(f32x4, lds128(c + 16));
            const f32x4 cu0 = __builtin_bit_cast(f32x4, lds128(c + 128)), cu1 = __builtin_bit_cast(f32x4, lds128(c + 144));
            const float mean = lds32(L_ST + (l & 3) * 8), rstd = lds32(L_ST + (l & 3) * 8 + 4);
            SB();
            const float r2 = rstd * rstd;
            float a8[8];
#pragma unroll
            for (int j = 0; j < 4; ++j) {
                a8[j] = fmaxf(g0[j] - mean * cg0[j], 0.f) * (u0[j] - mean * cu0[j]) * r2;
                a8[4 + j] = fmaxf(g1[j] - mean * cg1[j], 0.f) * (u1[j] - mean * cu1[j]) * r2;
            }
            const v4u af = {pk2(a8[0], a8[1]), pk2(a8[2], a8[3]), pk2(a8[4], a8[5]), pk2(a8[6], a8[7])};
            const f32x4 z = {0.f, 0.f, 0.f, 0.f};
            const f32x4 d0 = mfma16(df[0], af, z), d1 = mfma16(df[1], af, z), d2 = mfma16(df[2], af, z), d3 = mfma16(df[3], af, z);
            if (p16 < 4) {
                const unsigned o = W_OUTP + (unsigned)(fs * 4096 + w * 512 + (g >> 1) * 128 + p16 * 32 + (g & 1) * 16);
                gst128(rs, o, (v4u){pk2(d0.x, d0.y), pk2(d0.z, d0.w), pk2(d1.x, d1.y), pk2(d1.z, d1.w)});
                gst128(rs, o + 256, (v4u){pk2(d2.x, d2.y), pk2(d2.z, d2.w), pk2(d3.x, d3.y), pk2(d3.z, d3.w)});
            }
        }
        STAMP(11);
    }
    STAMP_OUT();
}

__global__ void __launch_bounds__(64) k_pre(unsigned* st) { if (threadIdx.x == 0 && blockIdx.x == 0) st[2048 * 16] = (unsigned)__builtin_amdgcn_s_memrealtime(); }
__global__ void __launch_bounds__(64) k_spin(const unsigned* st) {
    const unsigned now = (unsigned)__builtin_amdgcn_s_memrealtime();
    unsigned t0 = st[0]; int mn = 0, mx = 0;
    for (int i = threadIdx.x; i < 2048; i += 64) { mn = min(mn, (int)(st[i * 16] - t0)); if (i < 1024 && st[i * 16 + 15]) mx = max(mx, (int)(st[i * 16 + 15] - t0)); }
    for (int o = 32; o; o >>= 1) { mn = min(mn, __shfl_xor(mn, o)); mx = max(mx, __shfl_xor(mx, o)); }
    const int pre = (int)(st[2048 * 16] - t0), nw = (int)(now - t0);
    int m = 0;
    if (m > 5000) m = 5000; if (m < 0) m = 0;
    unsigned long long a = __builtin_amdgcn_s_memrealtime();
    while (__builtin_amdgcn_s_memrealtime() < a + (unsigned long long)m * 20) __builtin_amdgcn_s_sleep(2);
}
extern "C" void kernel_launch(void* const* d_in, const int* in_sizes, int n_in, void* d_out, int out_size, void* d_ws, size_t ws_size,
                              hipStream_t stream) {
    (void)in_sizes; (void)n_in; (void)out_size;
    unsigned char* arena = (unsigned char*)d_ws + ((ws_size - W_BYTES) & ~(size_t)4095);
    (void)hipFuncSetAttribute((const void*)fused_decode, hipFuncAttributeMaxDynamicSharedMemorySize, LDS_BYTES);
    k_pre<<<1, 64, 0, stream>>>((unsigned*)(arena + W_STAMP));
    fused_decode<<<256, 512, LDS_BYTES, stream>>>((const u16*)d_in[2], (const u16*)d_in[3], (const u16*)d_in[1], (const u16*)d_in[0], arena, (const u16*)d_in[4],
                                                  (const u16*)d_in[5], (const u16*)d_in[7], (const u16*)d_in[8], (const u16*)d_in[9], (u16*)d_out);
}
```

```cpp
#define STAMPS
#define Q_SLEEP 0
#include <hip/hip_runtime.h>
#include <stdint.h>

#define GAS __attribute__((address_space(1)))
#define LAS __attribute__((address_space(3)))
typedef unsigned short u16;
typedef unsigned v4u __attribute__((ext_vector_type(4)));
typedef unsigned v2u __attribute__((ext_vector_type(2)));
typedef float f32x4 __attribute__((ext_vector_type(4)));
typedef float f32x2 __attribute__((ext_vector_type(2)));
typedef float f32x16 __attribute__((ext_vector_type(16)));
typedef short s16x4 __attribute__((ext_vector_type(4)));
typedef __bf16 bf16x8 __attribute__((ext_vector_type(8)));
typedef __bf16 bf16x2 __attribute__((ext_vector_type(2)));

#define KNOB(n, v)
#define NT_POLICY " nt"
#define GU_POLICY ""
#define HOP1_SLEEP 2
#define UK_SLEEP "16"
#define KV_EARLY 0
#define KV_EARLY_AT 4
#define STAT_SLEEP 22
#define RETRY_SLEEP 1
#define PZ_SLEEP 0
#define PART_SLEEP2 20
#define STAT_GATE 0
#define QZ_SLEEP 14
#define PZC_SLEEP 8
#define KVPF 0
#define KVPF_SLEEP 0
#define PZ_AT 1
#define WO_DEPTH "1"
#define FFN_DEPTH 1
#define Q_FIRST 0
#define TAIL_SLEEP 40
#define FFN_SLEEP 40
#define PART_SLEEP 2
#define H4_SLEEP 6
#define H5_SLEEP 4
#define H6_SLEEP 8
#define CODE_BYTES "17400"

constexpr unsigned W_EPOCH = 0;
constexpr unsigned W_HINT = 4096;
constexpr unsigned W_QP = W_HINT + 32768;
constexpr unsigned W_QABS = W_QP + 262144;
constexpr unsigned W_STAT = W_QABS + 196608;
constexpr unsigned W_CTXP = W_STAT + 65536;
constexpr unsigned W_OUTP = W_CTXP + 131072;
constexpr unsigned W_HP = W_OUTP + 56 * 4096;
constexpr unsigned W_PZF = W_OUTP + 64 * 4096;
constexpr unsigned W_QZF = W_PZF + 4096;
constexpr unsigned W_PART = 1u << 20;
constexpr unsigned W_STAMP = W_PART + (4u << 20);
constexpr unsigned W_BYTES = 6u << 20;
static_assert(W_PZF + 16384 <= W_PART, "arena");
constexpr int LDS_BYTES = 163840;
constexpr unsigned POISON = 0xFFFFFFFFu;
constexpr int SPIN_CAP = 1 << 16;

__device__ __forceinline__ unsigned pk2(float a, float b) { f32x2 v = {a, b}; return __builtin_bit_cast(unsigned, __builtin_convertvector(v, bf16x2)); }
__device__ __forceinline__ float bflo(unsigned u) { return __uint_as_float(u << 16); }
__device__ __forceinline__ float bfhi(unsigned u) { return __uint_as_float(u & 0xFFFF0000u); }
__device__ __forceinline__ f32x4 mfma16(v4u a, v4u b, f32x4 c) { return __builtin_amdgcn_mfma_f32_16x16x32_bf16(__builtin_bit_cast(bf16x8, a), __builtin_bit_cast(bf16x8, b), c, 0, 0, 0); }
__device__ __forceinline__ f32x16 mfma32(v4u a, v4u b, f32x16 c) { return __builtin_amdgcn_mfma_f32_32x32x16_bf16(__builtin_bit_cast(bf16x8, a), __builtin_bit_cast(bf16x8, b), c, 0, 0, 0); }
__device__ __forceinline__ v4u lds128(unsigned a) { return *(LAS v4u*)(uintptr_t)a; }
__device__ __forceinline__ v2u ldstr(unsigned a) { return __builtin_bit_cast(v2u, __builtin_amdgcn_ds_read_tr16_b64_v4i16((LAS s16x4*)(uintptr_t)a)); }
__device__ __forceinline__ v4u ldstr2(unsigned a0, unsigned a1) { v2u x = ldstr(a0), y = ldstr(a1); return (v4u){x.x, x.y, y.x, y.y}; }
__device__ __forceinline__ void sts128(unsigned a, v4u v) { *(LAS v4u*)(uintptr_t)a = v; }
__device__ __forceinline__ void sts64(unsigned a, v2u v) { *(LAS v2u*)(uintptr_t)a = v; }
__device__ __forceinline__ void sts32(unsigned a, float v) { *(LAS float*)(uintptr_t)a = v; }
__device__ __forceinline__ float lds32(unsigned a) { return *(LAS float*)(uintptr_t)a; }
#define BAR() do { asm volatile("" ::: "memory"); __builtin_amdgcn_s_waitcnt(0xC07F); asm volatile("s_barrier" ::: "memory"); } while (0)
#define VMWAIT_(n) asm volatile("s_waitcnt vmcnt(" #n ")" ::: "memory")
#define VMWAIT(n) VMWAIT_(n)
#define CFENCE() asm volatile("" ::: "memory")
typedef __amdgpu_buffer_rsrc_t rsrc_t;
__device__ __forceinline__ v4u gld128(rsrc_t r, unsigned off, unsigned so = 0) { return __builtin_amdgcn_raw_buffer_load_b128(r, off, so, 16); }
__device__ __forceinline__ v2u gld64(rsrc_t r, unsigned off, unsigned so = 0) { return __builtin_amdgcn_raw_buffer_load_b64(r, off, so, 16); }
__device__ __forceinline__ void gst128(rsrc_t r, unsigned off, v4u v, unsigned so = 0) { __builtin_amdgcn_raw_buffer_store_b128(v, r, off, so, 16); }
__device__ __forceinline__ void gst64(rsrc_t r, unsigned off, v2u v, unsigned so = 0) { __builtin_amdgcn_raw_buffer_store_b64(v, r, off, so, 16); }
__device__ __forceinline__ void gst32(rsrc_t r, unsigned off, unsigned v) { __builtin_amdgcn_raw_buffer_store_b32(v, r, off, 0, 16); }
__device__ __forceinline__ v4u mksrd(const void* p, unsigned bytes) {
    const uint64_t a = (uint64_t)p;
    return (v4u){(unsigned)__builtin_amdgcn_readfirstlane((int)(unsigned)a), (unsigned)__builtin_amdgcn_readfirstlane((int)(unsigned)(a >> 32)) & 0xFFFFu, bytes, 0x00020000u};
}
__device__ __forceinline__ void dma(v4u srd, unsigned voff, unsigned soff, unsigned dst) {
    asm volatile("s_mov_b32 m0, %3\n\ts_nop 0\n\tbuffer_load_dwordx4 %0, %1, %2 offen" NT_POLICY " lds" :: "v"(voff), "s"(srd), "s"(soff), "s"(dst) : "memory");
}
__device__ __forceinline__ void dmag(const void* p, unsigned dst) {
    asm volatile("s_mov_b32 m0, %1\n\ts_nop 0\n\tglobal_load_lds_dwordx4 %0, off" GU_POLICY :: "v"(p), "s"(dst) : "memory");
}
__device__ __forceinline__ void dma_loop(v4u srd, unsigned voff, unsigned soff, unsigned sstep, unsigned dst, unsigned dstep, unsigned n) {
    asm volatile("s_mov_b32 m0, %4\n\ts_nop 3\n"
                 ".Ldl%=:\n\t"
                 "buffer_load_dwordx4 %2, %3, %0 offen" NT_POLICY " lds\n\t"
                 "s_add_u32 m0, m0, %6\n\ts_add_u32 %0, %0, %5\n\ts_sub_u32 %1, %1, 1\n\ts_cmp_lg_u32 %1, 0\n\ts_cbranch_scc1 .Ldl%="
                 : "+s"(soff), "+s"(n) : "v"(voff), "s"(srd), "s"(dst), "s"(sstep), "s"(dstep) : "scc", "memory");
}
__device__ __forceinline__ void dma_paced(v4u srd, unsigned voff, unsigned soff, unsigned sstep, unsigned dst, unsigned dstep, unsigned n) {
    asm volatile("s_mov_b32 m0, %4\n\ts_nop 3\n"
                 ".Ldp%=:\n\t"
                 "buffer_load_dwordx4 %2, %3, %0 offen" NT_POLICY " lds\n\t"
                 "s_waitcnt vmcnt(" WO_DEPTH ")\n\t"
                 "s_add_u32 m0, m0, %6\n\ts_add_u32 %0, %0, %5\n\ts_sub_u32 %1, %1, 1\n\ts_cmp_lg_u32 %1, 0\n\ts_cbranch_scc1 .Ldp%="
                 : "+s"(soff), "+s"(n) : "v"(voff), "s"(srd), "s"(dst), "s"(sstep), "s"(dstep) : "scc", "memory");
}
__device__ __forceinline__ float max3(float a, float b, float c) { float r; asm("v_max3_f32 %0, %1, %2, %3" : "=v"(r) : "v"(a), "v"(b), "v"(c)); return r; }
__device__ __forceinline__ float max2(float a, float b) { float r; asm("v_max_f32 %0, %1, %2" : "=v"(r) : "v"(a), "v"(b)); return r; }
__device__ __forceinline__ unsigned umax4(v4u v, unsigned m) { return max(max(max(v.x, v.y), max(v.z, v.w)), m); }
__device__ __forceinline__ float xsum16(float v) { auto r = __builtin_amdgcn_permlane16_swap(__float_as_uint(v), __float_as_uint(v), false, false); return __uint_as_float(r[0]) + __uint_as_float(r[1]); }
__device__ __forceinline__ float xsum32(float v) { auto r = __builtin_amdgcn_permlane32_swap(__float_as_uint(v), __float_as_uint(v), false, false); return __uint_as_float(r[0]) + __uint_as_float(r[1]); }
__device__ __forceinline__ float xmax16(float v) { auto r = __builtin_amdgcn_permlane16_swap(__float_as_uint(v), __float_as_uint(v), false, false); return fmaxf(__uint_as_float(r[0]), __uint_as_float(r[1])); }
__device__ __forceinline__ float xmax32(float v) { auto r = __builtin_amdgcn_permlane32_swap(__float_as_uint(v), __float_as_uint(v), false, false); return fmaxf(__uint_as_float(r[0]), __uint_as_float(r[1])); }
template <int CTRL> __device__ __forceinline__ float dpp(float v) { return __int_as_float(__builtin_amdgcn_update_dpp(0, __float_as_int(v), CTRL, 0xf, 0xf, true)); }
__device__ __forceinline__ float rsum16(float v) { v += dpp<0xB1>(v); v += dpp<0x4E>(v); v += dpp<0x141>(v); v += dpp<0x140>(v); return v; }
__device__ __forceinline__ float rmax16(float v) { v = fmaxf(v, dpp<0xB1>(v)); v = fmaxf(v, dpp<0x4E>(v)); v = fmaxf(v, dpp<0x141>(v)); v = fmaxf(v, dpp<0x140>(v)); return v; }
__device__ __forceinline__ float wsum(float v) { return xsum32(xsum16(rsum16(v))); }

#define STAMP_MASK 0xFFFF
#define STAMP(k) do { if (!((STAMP_MASK >> (k)) & 1)) break; __builtin_amdgcn_sched_barrier(0); unsigned long long t_; asm volatile("s_memrealtime %0\n\ts_waitcnt lgkmcnt(0)" : "=s"(t_) :: "memory"); \
    asm volatile("v_writelane_b32 %0, %1, " #k : "+v"(stv) : "s"((unsigned)t_)); __builtin_amdgcn_sched_barrier(0); } while (0)
#define STAMP_OUT() do { if (l < 16) ((GAS unsigned*)(ws + W_STAMP))[(wg * 8 + w) * 16 + l] = stv; } while (0)

__device__ __forceinline__ v4u selmask(int l, int r0) {
    const int j = (l & 15) - r0;
    const unsigned m = (j & 1) ? 0xFFFF0000u : 0x0000FFFFu;
    return (v4u){(j >> 1) == 0 ? m : 0u, (j >> 1) == 1 ? m : 0u, (j >> 1) == 2 ? m : 0u, (j >> 1) == 3 ? m : 0u};
}

__device__ __forceinline__ unsigned gld32(rsrc_t r, unsigned off) { return __builtin_amdgcn_raw_buffer_load_b32(r, off, 0, 16); }
template <int SLEEP> __device__ __forceinline__ void wait_flag(rsrc_t rs, unsigned off, unsigned tag) {
    _Pragma("unroll 1") for (int it = 0; it < SPIN_CAP; ++it) {
        CFENCE();
        const unsigned v = gld32(rs, off);
        if (__all(v == tag)) break;
        __builtin_amdgcn_s_sleep(SLEEP);
    }
    CFENCE();
}
#define SB() __builtin_amdgcn_sched_barrier(0)
#define PIN(x) asm volatile("" : "+v"(x))
template <int N, int NO> __device__ __forceinline__ void gissue(rsrc_t rs, const unsigned (&off)[NO], unsigned stride, v4u (&v)[N]) {
#pragma unroll
    for (int k = 0; k < N; ++k) v[k] = gld128(rs, off[k % NO], (k / NO) * stride);
}
template <int N, int NO> __device__ __forceinline__ void gcheck(rsrc_t rs, const unsigned (&off)[NO], unsigned stride, v4u (&v)[N]) {
    unsigned m = 0;
#pragma unroll
    for (int k = 0; k < N; ++k) m = umax4(v[k], m);
    if (__builtin_expect(!__all(m != POISON), 0)) {
        _Pragma("unroll 1") for (int it = 0; it < SPIN_CAP; ++it) {
            CFENCE();
            if (m == POISON) { gissue<N, NO>(rs, off, stride, v); m = 0;
#pragma unroll
                for (int k = 0; k < N; ++k) m = umax4(v[k], m); }
            if (__all(m != POISON)) break;
        }
    }
    CFENCE();
}
template <int N, int NO> __device__ __forceinline__ void gather(rsrc_t rs, const unsigned (&off)[NO], unsigned stride, v4u (&v)[N]) {
    CFENCE();
    gissue<N, NO>(rs, off, stride, v);
    gcheck<N, NO>(rs, off, stride, v);
}

__global__ void __launch_bounds__(512, 2)
fused_decode(const u16* __restrict__ wq, const u16* __restrict__ wuk, const u16* __restrict__ ckv_arg, const u16* __restrict__ x,
             unsigned char* __restrict__ ws_arg, const u16* __restrict__ wuv, const u16* __restrict__ wo, const u16* __restrict__ wg_,
             const u16* __restrict__ wu_, const u16* __restrict__ wd, u16* __restrict__ out) {
    extern __shared__ __attribute__((aligned(16))) unsigned char lds_[];
    unsigned stv = 0;
    STAMP(0);
    v4u xv;
    unsigned rawtag;
    unsigned char* ws;
    const u16* ckv;
    {
        unsigned t0, t1, t2, t3, pfs;
#define PDMA(V) "buffer_load_dwordx4 " V ", s[52:55], s58 offen" NT_POLICY " lds\n\t"
#define PSTEP "s_add_u32 m0, m0, 0x400\n\ts_add_u32 s58, s58, 0x1000\n\t"
        asm volatile(
            "s_load_dwordx8 s[40:47], %[ka], 0x0\n\t"
            "s_load_dwordx2 %[ws], %[ka], 0x20\n\t"
            "v_readfirstlane_b32 s50, %[tid]\n\t"
            "s_bfe_u32 s51, %[wg], 0x40003\n\t"
            "s_lshr_b32 s50, s50, 6\n\t"
            "s_cmp_lt_u32 s50, 4\n\t"
            "s_cbranch_scc1 .Ldm%=\n\t"
            "s_cmp_lt_u32 %[wg], 0x80\n\t"
            "s_cbranch_scc1 .Lxl%=\n\t"
            "s_getpc_b64 s[56:57]\n\t"
            "s_lshl_b32 s60, s51, 2\n\t"
            "s_add_u32 s60, s60, s50\n\t"
            "s_mul_i32 s60, s60, 320\n\t"
            "s_sub_u32 s60, s60, 1280\n\t"
            "s_min_u32 s60, s60, " CODE_BYTES "\n\t"
            "s_add_u32 s56, s56, s60\n\t"
            "s_addc_u32 s57, s57, 0\n\t"
            "s_load_dword %[pf], s[56:57], 0x0\n\t"
            "s_load_dword %[pf], s[56:57], 0x40\n\t"
            "s_load_dword %[pf], s[56:57], 0x80\n\t"
            "s_load_dword %[pf], s[56:57], 0xc0\n\t"
            "s_load_dword %[pf], s[56:57], 0x100\n\t"
            "s_waitcnt lgkmcnt(0)\n\t"
            "s_load_dword %[tag], %[ws], 0x0\n\t"
            "s_branch .Lpe%=\n"
            ".Lxl%=:\n\t"
            "v_lshlrev_b32 %[t1], 4, %[tid]\n\t"
            "s_waitcnt lgkmcnt(0)\n\t"
            "global_load_dwordx4 %[xv], %[t1], s[46:47] offset:-4096\n\t"
            "s_load_dword %[tag], %[ws], 0x0\n\t"
            "s_branch .Lpe%=\n"
            ".Ldm%=:\n\t"
            "v_bfe_u32 %[t2], %[tid], 4, 2\n\t"
            "v_and_b32 %[t3], 15, %[tid]\n\t"
            "s_and_b32 s59, %[wg], 7\n\t"
            "s_lshl_b32 s62, s50, 15\n\t"
            "s_lshl_b32 s58, s59, 10\n\t"
            "s_lshr_b32 s60, s51, 2\n\t"
            "s_lshl_b32 s60, s60, 20\n\t"
            "s_and_b32 s61, s51, 3\n\t"
            "s_lshl_b32 s61, s61, 8\n\t"
            "s_add_u32 s58, s58, s62\n\t"
            "s_add_u32 s60, s60, s61\n\t"
            "s_add_u32 s58, s58, s60\n\t"
            "v_lshlrev_b32 %[t0], 2, %[t2]\n\t"
            "v_xor_b32 %[t1], s50, %[t3]\n\t"
            "v_xor_b32 %[t1], %[t0], %[t1]\n\t"
            "v_lshlrev_b32 %[t0], 13, %[t2]\n\t"
            "v_lshl_add_u32 %[t0], %[t1], 4, %[t0]\n\t"
            "s_lshl_b32 s60, s50, 10\n\t"
            "s_add_u32 m0, s60, 0x20000\n\t"
            "s_mov_b32 s54, 0x400000\n\t"
            "s_mov_b32 s55, 0x20000\n\t"
            "s_mov_b32 s61, 8\n\t"
            "s_cmp_lt_u32 %[wg], 0x80\n\t"
            "s_waitcnt lgkmcnt(0)\n\t"
            "s_cselect_b32 s52, s40, s42\n\t"
            "s_cselect_b32 s53, s41, s43\n\t"
            "s_and_b32 s53, s53, 0xffff\n\t"
            "s_mov_b64 %[kv], s[44:45]\n\t"
            "s_load_dword %[tag], %[ws], 0x0\n\t"
            "s_cmp_lt_u32 %[wg], 0x80\n\t"
            "s_cbranch_scc1 .Lf1%=\n\t"
            "s_cmp_lg_u32 s50, 0\n\t"
            "s_cbranch_scc1 .Lf0%=\n\t"
            "s_lshl_b32 s60, s59, 12\n\t"
            "s_and_b32 s62, s51, 3\n\t"
            "s_lshl_b32 s62, s62, 10\n\t"
            "s_add_u32 s60, s60, s62\n\t"
            "s_lshr_b32 s62, s51, 2\n\t"
            "s_lshl_b32 s62, s62, 8\n\t"
            "s_add_u32 s60, s60, s62\n\t"
            "s_add_u32 s60, s60, %[oq]\n\t"
            "v_lshlrev_b32 %[t2], 15, %[t2]\n\t"
            "v_lshl_add_u32 %[t2], %[t3], 4, %[t2]\n\t"
            "v_add_u32 %[t2], s60, %[t2]\n\t"
            "global_store_dwordx4 %[t2], %[pz], %[ws] sc1\n\t"
            "s_lshl_b32 s60, %[wg], 2\n\t"
            "s_add_u32 s60, s60, %[oz]\n\t"
            "v_mov_b32 %[t2], s60\n\t"
            "s_waitcnt vmcnt(0) lgkmcnt(0)\n\t"
            "s_add_u32 s60, %[tag], 0x01010101\n\t"
            "v_mov_b32 %[t3], s60\n\t"
            "s_mov_b64 exec, 1\n\t"
            "global_store_dword %[t2], %[t3], %[ws] sc1\n\t"
            "s_mov_b64 exec, -1\n\t"
            "s_branch .Lf1%=\n"
            ".Lf0%=:\n\t"
            "s_sleep " UK_SLEEP "\n"
            ".Lf1%=:\n\t"
            PDMA("%[t0]")
            "s_add_u32 m0, m0, 0x1000\n\ts_add_u32 s58, s58, 0x20000\n\ts_sub_u32 s61, s61, 1\n\ts_cmp_lg_u32 s61, 0\n\ts_cbranch_scc1 .Lf1%=\n"
            ".Lpe%=:"
            : [xv] "=&v"(xv), [tag] "=&s"(rawtag), [ws] "=&s"(ws), [kv] "=&s"(ckv), [pf] "=&s"(pfs), [t0] "=&v"(t0), [t1] "=&v"(t1), [t2] "=&v"(t2), [t3] "=&v"(t3)
            : [ka] "s"(__builtin_amdgcn_kernarg_segment_ptr()), [tid] "v"(threadIdx.x), [wg] "s"(blockIdx.x), [pz] "v"((v4u){POISON, POISON, POISON, POISON}),
              [oq] "i"(W_QABS), [oz] "i"(W_QZF - 512)
            : "s40", "s41", "s42", "s43", "s44", "s45", "s46", "s47", "s50", "s51", "s52", "s53", "s54", "s55", "s56", "s57", "s58", "s59", "s60", "s61", "s62", "scc", "memory");
    }
    SB();
    const int wg = blockIdx.x, l = threadIdx.x & 63, w = __builtin_amdgcn_readfirstlane(threadIdx.x >> 6);
    const int xc = wg & 7, ix = (wg >> 3) & 15;
    const int g = l >> 4, p16 = l & 15;
    const int hh = 4 * xc + (ix & 3), qd = ix >> 2;
    const int ab = xc >> 1, arow = xc * 16 + ix;
    unsigned tag;
#define GET_TAG() asm volatile("s_waitcnt lgkmcnt(0)\n\ts_add_u32 %0, %1, 0x01010101" : "=s"(tag) : "s"(rawtag) : "scc", "memory")
    const rsrc_t rs = __builtin_amdgcn_make_buffer_rsrc(ws, 0, W_BYTES, 0x00020000);
    const unsigned trq = (unsigned)(p16 >> 2), trp = (unsigned)(l & 3);
    const unsigned tra = (8 * g + trq) * 256 + (((2 * w + (trp >> 1)) ^ ((trq << 2) | ((2 * g) & 3))) * 16) + 8 * (trp & 1);

    if (__builtin_expect(wg < 128, 1)) {
        constexpr unsigned L_Q = 131072, L_XN = 98304, L_MX = 0, L_P = 1024, L_LS = 9216, L_CL = 12288, L_WUV = 32768;
        auto kvload = [&](unsigned cb, unsigned n) {
            const unsigned v0 = (unsigned)g * 1024u + (unsigned)((p16 ^ (2 * g)) * 16);
            unsigned so = (unsigned)(arow * 128 + 32 * w) * 1024u + cb * 256;
#define KDMA(V) "buffer_load_dwordx4 " V ", %6, %0 offen" NT_POLICY " lds\n\t"
#define KSTEP "s_add_u32 m0, m0, 0x400\n\ts_add_u32 %0, %0, 0x1000\n\t"
            asm volatile("s_mov_b32 m0, %7\n\ts_nop 3\n"
                         ".Lkv%=:\n\t"
                         KDMA("%2") KSTEP KDMA("%3") KSTEP KDMA("%4") KSTEP KDMA("%5") KSTEP KDMA("%2") KSTEP KDMA("%3") KSTEP KDMA("%4") KSTEP KDMA("%5")
                         "s_add_u32 m0, m0, 25600\n\ts_add_u32 %0, %0, 0xffff9100\n\ts_sub_u32 %1, %1, 1\n\ts_cmp_lg_u32 %1, 0\n\ts_cbranch_scc1 .Lkv%="
                         : "+s"(so), "+s"(n) : "v"(v0), "v"(v0 ^ 128u), "v"(v0 ^ 16u), "v"(v0 ^ 144u), "s"(mksrd(ckv, 16777216u)), "s"(cb * 32768 + w * 8192) : "scc", "memory");
        };
        if (w < 4) {
            GET_TAG();
            STAMP(1);
            VMWAIT(0);
        } else {
            GET_TAG();
            STAMP(1);
            asm volatile("s_waitcnt vmcnt(0)" : "+v"(xv) :: "memory");
            float v[8] = {bflo(xv.x), bfhi(xv.x), bflo(xv.y), bfhi(xv.y), bflo(xv.z), bfhi(xv.z), bflo(xv.w), bfhi(xv.w)};
            const float mean = wsum(((v[0] + v[1]) + (v[2] + v[3])) + ((v[4] + v[5]) + (v[6] + v[7]))) * (1.f / 512.f);
            float ss = 0.f;
#pragma unroll
            for (int j = 0; j < 8; ++j) { v[j] -= mean; ss += v[j] * v[j]; }
            const float rstd = rsqrtf(wsum(ss) * (1.f / 512.f) + 1e-5f);
            if (g == qd) sts128(L_XN + (w - 4) * 320 + p16 * 16, (v4u){pk2(v[0] * rstd, v[1] * rstd), pk2(v[2] * rstd, v[3] * rstd), pk2(v[4] * rstd, v[5] * rstd), pk2(v[6] * rstd, v[7] * rstd)});
        }
        unsigned qpo = p16 < 4 ? W_QP + (unsigned)(((hh * 4 + qd) * 8 + w) * 256 + p16 * 64 + g * 16) : W_BYTES, qa0 = L_Q + tra, qa1 = L_Q + (tra ^ 16) + 1024, xna = L_XN + (l & 3) * 320 + g * 16;
        PIN(qpo); PIN(qa0); PIN(qa1); PIN(xna);
        STAMP(12);
        BAR();
        {
            v4u af[4], bf[4];
#pragma unroll
            for (int s = 0; s < 4; ++s) { af[s] = ldstr2(qa0 + s * 8192, qa1 + s * 8192); bf[s] = lds128(xna + s * 64); SB(); }
            SB();
            f32x4 acc = {0.f, 0.f, 0.f, 0.f};
#pragma unroll
            for (int s = 0; s < 4; ++s) acc = mfma16(af[s], bf[s], acc);
            gst128(rs, qpo, (v4u){pk2(acc.x, acc.y), tag, pk2(acc.z, acc.w), tag});
        }
        STAMP(11);
        BAR();
        if (w < 4) {
            kvload(KV_EARLY, 4 - KV_EARLY - Q_FIRST);
            const int r = l & 31, hf = l >> 5;
            unsigned fa[8];
#pragma unroll
            for (int s = 0; s < 8; ++s) fa[s] = (unsigned)(r * 256 + (((2 * s + hf) ^ (((r & 7) << 1) | ((r >> 3) & 1))) * 16)) + w * 8192;
            v4u kf[32];
            VMWAIT(16);
#pragma unroll
            for (int s = 0; s < 16; ++s) kf[s] = lds128(fa[s & 7] + (s >> 3) * 32768);
#pragma unroll
            for (int s = 0; s < 8; ++s) { fa[s] += 65536; PIN(fa[s]); }
            BAR();
            STAMP(2);
            VMWAIT(8);
#pragma unroll
            for (int s = 16; s < 24; ++s) kf[s] = lds128(fa[s & 7]);
            VMWAIT(0);
            STAMP(3);
#pragma unroll
            for (int s = 24; s < 32; ++s) kf[s] = lds128(fa[s & 7] + 32768);
            unsigned pa = L_P + (unsigned)((w * 2 + (r >> 4)) * 1024 + hf * 256 + (r & 15) * 16), mxw = L_MX + r * 16 + w * 4, mxr = L_MX + r * 16;
            unsigned lsw = L_LS + r * 32 + hf * 16 + w * 4;
#pragma unroll
            for (int s = 0; s < 8; ++s) { fa[s] += L_Q - 65536 - w * 8192; PIN(fa[s]); }
            PIN(pa); PIN(mxw); PIN(mxr); PIN(lsw);
            __builtin_amdgcn_s_setprio(2);
            BAR();
            STAMP(4);
            f32x16 sa;
#pragma unroll
            for (int j = 0; j < 16; ++j) sa[j] = 0.f;
            {
                constexpr int QD = 6;
                v4u qf[32];
#pragma unroll
                for (int s = 0; s < QD; ++s) { qf[s] = lds128(fa[s & 7] + (s >> 3) * 8192); SB(); }
#pragma unroll
                for (int s = 0; s < 32; ++s) {
                    sa = mfma32(kf[s], qf[s], sa);
                    SB();
                    if (s + QD < 32) qf[s + QD] = lds128(fa[(s + QD) & 7] + ((s + QD) >> 3) * 8192);
                    SB();
                }
            }
            float mw;
            {
                float m, t1, t2;
                asm volatile("s_nop 11\n\tv_max3_f32 %0, %3, %4, %5\n\tv_max3_f32 %1, %6, %7, %8\n\tv_max3_f32 %2, %9, %10, %11\n\t"
                             "v_max3_f32 %0, %0, %12, %13\n\tv_max3_f32 %1, %1, %14, %15\n\tv_max3_f32 %2, %2, %16, %17\n\tv_max3_f32 %0, %0, %1, %18\n\tv_max_f32 %0, %0, %2"
                             : "=&v"(m), "=&v"(t1), "=&v"(t2)
                             : "v"(sa[0]), "v"(sa[1]), "v"(sa[2]), "v"(sa[3]), "v"(sa[4]), "v"(sa[5]), "v"(sa[6]), "v"(sa[7]), "v"(sa[8]), "v"(sa[9]), "v"(sa[10]), "v"(sa[11]),
                               "v"(sa[12]), "v"(sa[13]), "v"(sa[14]), "v"(sa[15]));
                auto x = __builtin_amdgcn_permlane32_swap(__float_as_uint(m), __float_as_uint(m), false, false);
                mw = max2(__uint_as_float(x[0]), __uint_as_float(x[1]));
            }
            sts32(mxw, mw);
            f32x2 e[8];
#pragma unroll
            for (int j = 0; j < 8; ++j) e[j] = (f32x2){sa[2 * j] - mw, sa[2 * j + 1] - mw};
#pragma unroll
            for (int j = 0; j < 8; ++j) e[j] = (f32x2){__builtin_amdgcn_exp2f(e[j].x), __builtin_amdgcn_exp2f(e[j].y)};
            {
                const f32x2 t = ((e[0] + e[1]) + (e[2] + e[3])) + ((e[4] + e[5]) + (e[6] + e[7]));
                sts32(lsw, t.x + t.y);
            }
            STAMP(5);
            BAR();
            const f32x4 mv = __builtin_bit_cast(f32x4, lds128(mxr));
            const float f = __builtin_amdgcn_exp2f(mw - max2(max3(mv.x, mv.y, mv.z), mv.w));
#pragma unroll
            for (int j = 0; j < 4; ++j) e[j] *= f;
            sts128(pa, (v4u){pk2(e[0].x, e[0].y), pk2(e[1].x, e[1].y), pk2(e[2].x, e[2].y), pk2(e[3].x, e[3].y)});
#pragma unroll
            for (int j = 4; j < 8; ++j) e[j] *= f;
            sts128(pa + 512, (v4u){pk2(e[4].x, e[4].y), pk2(e[5].x, e[5].y), pk2(e[6].x, e[6].y), pk2(e[7].x, e[7].y)});
            BAR();
            __builtin_amdgcn_s_setprio(0);
            STAMP(6);
        } else {
            const int ow = w - 4;
            const unsigned va = (unsigned)(ow * 32768 + (16 * (g >> 1) + 4 * (g & 1) + trq) * 256 + (((trp >> 1) ^ (8 * (g & 1) + 2 * trq)) * 16) + 8 * (trp & 1));
            v4u vf[4][8];
            auto vread = [&]() {
#pragma unroll
                for (int kg = 0; kg < 4; ++kg)
#pragma unroll
                    for (int ct = 0; ct < 8; ++ct) vf[kg][ct] = ldstr2((va ^ (ct * 32)) + kg * 8192, (va ^ (ct * 32 + 16)) + kg * 8192 + 2048);
            };
            {
                const unsigned off[1] = {W_QABS + (unsigned)((ab * 32 + 8 * ow) * 1024 + l * 16)};
                const unsigned qa = L_Q + (unsigned)(g * 8192 + 8 * ow * 256 + ((p16 ^ (ow & 1)) * 16));
                if (ow == 3) {
                    __builtin_amdgcn_s_setprio(3);
                    __builtin_amdgcn_s_sleep(QZ_SLEEP);
                    _Pragma("unroll 1") for (int it = 0; it < SPIN_CAP; ++it) {
                        CFENCE();
                        const v2u z = gld64(rs, W_QZF + l * 8);
                        if (__all(z.x == tag && z.y == tag)) break;
                        __builtin_amdgcn_s_sleep(8);
                    }
                    __builtin_amdgcn_s_setprio(0);
                    STAMP(13);
                }
                BAR();
                STAMP(2);
                __builtin_amdgcn_s_sleep(Q_SLEEP);
                _Pragma("unroll 1") for (int it = 0;; ++it) {
                    CFENCE();
                    v4u qv[8];
#pragma unroll
                    for (int k = 0; k < 8; ++k) { qv[k] = gld128(rs, off[0], k * 1024); SB(); }
                    if (ow < 2 && it == 0) vread();
                    unsigned m = 0;
#pragma unroll
                    for (int k = 0; k < 8; ++k) { sts128((qa ^ (k * 32)) + k * 256, qv[k]); m = umax4(qv[k], m); SB(); }
                    if (it == 0) STAMP(3);
                    if (__all(m != POISON) || it >= SPIN_CAP) break;
                }
                STAMP(4);
            }
            BAR();
            const unsigned fo = W_PZF + (unsigned)(ow * 64 + l) * 16u;
            v4u f;
            if (ow >= 2) {
                vread();
            } else {
                __builtin_amdgcn_s_sleep(PZC_SLEEP);
                asm volatile("s_nop 4\n\tbuffer_load_dwordx4 %0, %1, %2, 0 offen sc1" : "=v"(f) : "v"(fo), "s"(mksrd(ws, W_BYTES)) : "memory");
                const v4u srd = mksrd(wuv, 4194304);
                const unsigned vo = (unsigned)g * (16u * 8192u) + (unsigned)((p16 ^ (g << 2) ^ ow) * 16), so = (unsigned)(128 * qd + 64 * ow) * 8192u + hh * 256;
#pragma unroll
                for (int k = 0; k < 4; ++k) dma_loop(srd, vo ^ ((k & 1) * 32), so + 4 * k * 8192, 8192, L_WUV + (2 * k + ow) * 1024, 8192, 4);
            }
            unsigned po = W_PART + (unsigned)(((16 * g + ow) * 128 + arow) * 256 + p16 * 16), pfa = L_P + l * 16;
            PIN(po); PIN(pfa);
            STAMP(5);
            BAR();
            if (ow < 2) {
                asm volatile("s_waitcnt vmcnt(16)" : "+v"(f) :: "memory");
                bool ok = f.x == tag && f.y == tag && f.z == tag && f.w == tag;
                if (__builtin_expect(!__all(ok), 0)) {
                    _Pragma("unroll 1") for (int it = 0; it < SPIN_CAP; ++it) {
                        __builtin_amdgcn_s_sleep(2);
                        CFENCE();
                        const v4u f2 = gld128(rs, ok ? W_BYTES : fo);
                        ok = ok || (f2.x == tag && f2.y == tag && f2.z == tag && f2.w == tag);
                        if (__all(ok)) break;
                    }
                }
            } else {
                if (ow == 2) {
                    const f32x4 mv = __builtin_bit_cast(f32x4, lds128(L_MX + (l & 31) * 16));
                    const f32x4 lv = __builtin_bit_cast(f32x4, lds128(L_LS + (l & 31) * 32)) + __builtin_bit_cast(f32x4, lds128(L_LS + (l & 31) * 32 + 16));
                    const float M = max2(max3(mv.x, mv.y, mv.z), mv.w);
                    const float L = (lv.x * __builtin_amdgcn_exp2f(mv.x - M) + lv.y * __builtin_amdgcn_exp2f(mv.y - M)) + (lv.z * __builtin_amdgcn_exp2f(mv.z - M) + lv.w * __builtin_amdgcn_exp2f(mv.w - M));
                    gst128(rs, l < 32 ? W_STAT + (unsigned)(arow * 512 + l * 16) : W_BYTES, (v4u){__float_as_uint(M), tag, __float_as_uint(L), tag});
                }
            }
            STAMP(14);
            __builtin_amdgcn_s_setprio(1);
            BAR();
            STAMP(6);
            v4u pf[8];
#pragma unroll
            for (int k = 0; k < 8; ++k) { pf[k] = lds128(pfa + ((k & 3) * 2 + (k >> 2)) * 1024); SB(); }
            f32x4 acc[2][8];
#pragma unroll
            for (int ct = 0; ct < 8; ++ct) { acc[0][ct] = (f32x4){0.f, 0.f, 0.f, 0.f}; acc[1][ct] = (f32x4){0.f, 0.f, 0.f, 0.f}; }
#pragma unroll
            for (int kg = 0; kg < 4; ++kg)
#pragma unroll
                for (int ct = 0; ct < 8; ++ct) acc[0][ct] = mfma16(pf[kg], vf[kg][ct], acc[0][ct]);
            SB();
            v4u oa[2][4];
#define PACK(h, j) oa[h][j] = (v4u){pk2(acc[h][0][j], acc[h][1][j]), pk2(acc[h][2][j], acc[h][3][j]), pk2(acc[h][4][j], acc[h][5][j]), pk2(acc[h][6][j], acc[h][7][j])}
#pragma unroll
            for (int kg = 0; kg < 4; ++kg) {
#pragma unroll
                for (int ct = 0; ct < 8; ++ct) {
                    acc[1][ct] = mfma16(pf[4 + kg], vf[kg][ct], acc[1][ct]);
                    if (kg < 2 && (ct & 3) == 1) { SB(); PACK(0, 2 * kg + (ct >> 2)); SB(); }
                    if (kg < 2 && (ct & 3) == 3) { SB(); gst128(rs, po, oa[0][2 * kg + (ct >> 2)], (2 * kg + (ct >> 2)) * 131072); SB(); }
                }
            }
            SB();
#pragma unroll
            for (int j = 0; j < 4; ++j) { PACK(1, j); gst128(rs, po, oa[1][j], 2097152 + j * 131072); SB(); }
            asm volatile("" :: "v"(oa[0][0]), "v"(oa[0][1]), "v"(oa[0][2]), "v"(oa[0][3]), "v"(oa[1][0]), "v"(oa[1][1]), "v"(oa[1][2]), "v"(oa[1][3]));
            __builtin_amdgcn_s_setprio(0);
            STAMP(8);
            VMWAIT(8);
        }
        {
            unsigned clr = L_CL + (l & 3) * 272 + g * 64;
            unsigned cto = p16 < 4 ? W_CTXP + (unsigned)((hh * 4 + qd) * 1024 + w * 128 + p16 * 32 + g * 8) : W_BYTES;
            PIN(clr); PIN(cto);
            v4u uf[4];
            if (w < 4) {
                const unsigned sto = W_STAT + (unsigned)((w * 32 + (l & 31)) * 512 + hh * 16);
                const unsigned pvo[1] = {W_PART + (unsigned)(((hh * 4 + qd) * 128 + w * 32) * 256 + l * 16)};
                BAR();
                STAMP(7);
                v4u st = gld128(rs, sto);
                SB();
#pragma unroll
                for (int s = 0; s < 4; ++s) uf[s] = ldstr2(L_WUV + tra + s * 8192, L_WUV + (tra ^ 16) + 1024 + s * 8192);
                const v4u sm = selmask(l, 0);
                unsigned cla = L_CL + w * 272 + p16 * 16 + (g & 1) * 8 + (g >> 1) * 4096;
                unsigned hto = threadIdx.x == 0 ? W_HINT + wg * 128 : W_BYTES;
                PIN(cla); PIN(hto);
                v4u pv[8];
                SB();
                __builtin_amdgcn_s_sleep(PART_SLEEP);
                SB();
                gissue<8, 1>(rs, pvo, 1024, pv);
                SB();
                if (__builtin_expect(!__all(st.y == tag && st.w == tag), 0)) {
                    _Pragma("unroll 1") for (int it = 0; it < SPIN_CAP; ++it) {
                        CFENCE();
                        if (st.y != tag || st.w != tag) st = gld128(rs, sto);
                        if (__all(st.y == tag && st.w == tag)) break;
                    }
                }
                STAMP(14);
                const float ms = __uint_as_float(st.x);
                const float Mx = xmax16(rmax16(ms));
                const unsigned cb2 = pk2(__builtin_amdgcn_exp2f(ms - Mx), __builtin_amdgcn_exp2f(ms - Mx));
                float rl = __builtin_amdgcn_rcpf(xsum16(rsum16(bflo(cb2) * __uint_as_float(st.z))));
                v4u cf[8];
#pragma unroll
                for (int t = 0; t < 8; ++t) { const unsigned c = (unsigned)__builtin_amdgcn_ds_bpermute((4 * t + g) * 4, (int)cb2); cf[t] = sm & c; PIN(cf[t]); }
                PIN(rl);
                STAMP(13);
                SB();
                {
                    unsigned m = 0;
#pragma unroll
                    for (int k = 0; k < 8; ++k) m = umax4(pv[k], m);
                    STAMP(8);
                    if (__builtin_expect(!__all(m != POISON), 0)) {
                        _Pragma("unroll 1") for (int it = 0; it < SPIN_CAP; ++it) {
                            CFENCE();
#pragma unroll
                            for (int k = 0; k < 8; ++k) if (umax4(pv[k], 0u) == POISON) pv[k] = gld128(rs, pvo[0], k * 1024);
                            m = 0;
#pragma unroll
                            for (int k = 0; k < 8; ++k) m = umax4(pv[k], m);
                            if (__all(m != POISON)) break;
                        }
                    }
                    CFENCE();
                }
                STAMP(9);
                f32x4 acc = {0.f, 0.f, 0.f, 0.f};
#pragma unroll
                for (int t = 0; t < 8; ++t) acc = mfma16(cf[t], pv[t], acc);
                gst64(rs, hto, (v2u){tag, tag});
                SB();
                sts64(cla, (v2u){pk2(acc.x * rl, acc.y * rl), pk2(acc.z * rl, acc.w * rl)});
            } else {
                BAR();
#pragma unroll
                for (int s = 0; s < 4; ++s) uf[s] = ldstr2(L_WUV + tra + s * 8192, L_WUV + (tra ^ 16) + 1024 + s * 8192);
            }
            BAR();
            v4u bf[4];
#pragma unroll
            for (int s = 0; s < 4; ++s) { bf[s] = lds128(clr + s * 16); SB(); }
            f32x4 c4 = {0.f, 0.f, 0.f, 0.f};
#pragma unroll
            for (int s = 0; s < 4; ++s) c4 = mfma16(uf[s], bf[s], c4);
            gst64(rs, cto, (v2u){pk2(c4.x, c4.y), pk2(c4.z, c4.w)});
        }
        STAMP(10);
        if (ix < 4 && w == 4) {
            const int r = xc * 4 + ix, oi = ((l >> 1) & 3) * 512 + r * 16 + (l & 1) * 8 + (g & 1) * 4;
            const v2u xr = *(const GAS v2u*)(x + oi);
            const v4u sel0 = selmask(l, 0) & 0x3F803F80u;
            wait_flag<1>(rs, W_HINT + (unsigned)(wg + 128 + 64) * 128u, tag);
            __builtin_amdgcn_s_sleep(H6_SLEEP);
            v4u v[8];
            { const unsigned o[1] = {W_OUTP + (unsigned)((l >> 3) * 4096 + r * 128 + (l & 7) * 16)}; gather<8, 1>(rs, o, 8 * 4096, v); }
            f32x4 acc = {0.f, 0.f, 0.f, 0.f};
#pragma unroll
            for (int t = 0; t < 8; ++t) acc = mfma16(sel0, v[t], acc);
            const float o0 = acc.x + dpp<0x128>(acc.x) + bflo(xr.x), o1 = acc.y + dpp<0x128>(acc.y) + bfhi(xr.x);
            const float o2 = acc.z + dpp<0x128>(acc.z) + bflo(xr.y), o3 = acc.w + dpp<0x128>(acc.w) + bfhi(xr.y);
            if (g < 2 && p16 < 8) *(GAS v2u*)(out + oi) = (v2u){pk2(o0, o1), pk2(o2, o3)};
            if (r == 0 && l == 0) gst32(rs, W_EPOCH, tag);
            STAMP(15);
        }
        STAMP_OUT();
        return;
    }

    const bool isO = ix < 8;
    const int fs = xc * 7 + (ix - 8);
    {
        constexpr unsigned L_WUK = 131072, L_QL = 122880, L_QA = L_QL + 6144;
        const v4u pz = {POISON, POISON, POISON, POISON};
        const unsigned t = threadIdx.x;
        auto pzfill = [&]() {
#pragma unroll
            for (int k = 0; k < 8; ++k) gst128(rs, W_PART + (unsigned)(wg - 128) * 32768u + (t - 256) * 16, pz, k * 4096);
        };
        if (w < 4) {
            gst128(rs, w == 0 ? W_CTXP + (unsigned)(hh * 4 + qd) * 1024u + l * 16 : W_BYTES, pz);
            gst128(rs, isO ? (t < 32 ? W_HP + (unsigned)(xc * 4096 + ix * 512) + t * 16 : W_BYTES) : (ix < 15 ? W_OUTP + (unsigned)fs * 4096u + t * 16 : W_BYTES), pz);
        }
        auto kvpf = [&](unsigned cb) {
            unsigned so = (unsigned)(arow * 128 + 32 * (w & 3)) * 1024u + cb * 256, n = 8;
            asm volatile("s_mov_b32 m0, %4\n\ts_nop 3\n"
                         ".Lpf%=:\n\t"
                         "buffer_load_dwordx4 %2, %3, %0 offen lds\n\t"
                         "s_add_u32 %0, %0, 0x1000\n\ts_sub_u32 %1, %1, 1\n\ts_cmp_lg_u32 %1, 0\n\ts_cbranch_scc1 .Lpf%="
                         : "+s"(so), "+s"(n) : "v"((unsigned)g * 1024u + p16 * 16), "s"(mksrd(ckv_arg, 16777216u)), "s"(110592 + w * 1024) : "scc", "memory");
        };
        if (w >= 4 && KVPF >= 1) { __builtin_amdgcn_s_sleep(KVPF_SLEEP); kvpf(3); }
        unsigned off = W_QP + (unsigned)(((hh * 4 + g) * 8 + 2 * w) * 256 + p16 * 16), qlw = L_QL + (g * 4 + (p16 >> 2)) * 272 + w * 64 + (p16 & 3) * 8, qlr = L_QL + p16 * 272 + g * 64;
        unsigned qaw = L_QA + (l & 3) * 256 + w * 32 + g * 8, qar = L_QA + l * 16;
        unsigned qo = W_QABS + (unsigned)((g * 32 + hh) * 1024 + qd * 256 + p16 * 16);
        PIN(off); PIN(qlw); PIN(qlr); PIN(qaw); PIN(qar); PIN(qo);
        GET_TAG();
        if (w < 4) VMWAIT(0);
        else if (PZ_AT == 0) { __builtin_amdgcn_s_sleep(PZ_SLEEP); pzfill(); }
        STAMP(1);
        BAR();
        v4u af[4];
        {
            const unsigned a = L_WUK + (16 * w + p16) * 256 + (((4 * g) ^ (((p16 & 3) << 2) | (p16 >> 2))) * 16);
#pragma unroll
            for (int s = 0; s < 4; ++s) af[s] = lds128(a ^ (s * 16));
        }
        STAMP(2);
        if (w < 4) {
            __builtin_amdgcn_s_sleep(HOP1_SLEEP);
            CFENCE();
            STAMP(3);
            v4u v = gld128(rs, off), u = gld128(rs, off + 256);
            if (__builtin_expect(!__all(v.y == tag && v.w == tag && u.y == tag && u.w == tag), 0)) {
                STAMP(4);
                _Pragma("unroll 1") for (int it = 0; it < SPIN_CAP; ++it) {
                    CFENCE();
                    if (v.y != tag || v.w != tag || u.y != tag || u.w != tag) { v = gld128(rs, off); u = gld128(rs, off + 256); }
                    if (__all(v.y == tag && v.w == tag && u.y == tag && u.w == tag)) break;
                }
            }
            STAMP(5);
            sts64(qlw, (v2u){v.x, v.z});
            sts64(qlw + 32, (v2u){u.x, u.z});
        }
        BAR();
        STAMP(14);
        {
            v4u bf[4];
#pragma unroll
            for (int s = 0; s < 4; ++s) { bf[s] = lds128(qlr + s * 16); SB(); }
            f32x4 acc = {0.f, 0.f, 0.f, 0.f};
#pragma unroll
            for (int s = 0; s < 4; ++s) acc = mfma16(af[s], bf[s], acc);
            acc *= 0.08838834764831845f * 1.4426950408889634f;
#pragma unroll
            for (int j = 0; j < 4; ++j) acc[j] += dpp<0x128>(acc[j]);
#pragma unroll
            for (int j = 0; j < 4; ++j) acc[j] += dpp<0x124>(acc[j]);
            sts64(qaw, (v2u){pk2(acc.x, acc.y), pk2(acc.z, acc.w)});
        }
        STAMP(13);
        BAR();
        if (w == 0) gst128(rs, qo, lds128(qar));
        STAMP(6);
        if (w < 4 && KVPF >= 2) kvpf(2);
        if (w >= 4) {
            if (PZ_AT == 1) { __builtin_amdgcn_s_sleep(PZ_SLEEP); pzfill(); }
            VMWAIT(0);
            if (l == 0) gst32(rs, W_PZF + (unsigned)(wg - 128) * 16u + (w - 4) * 4, tag);
            STAMP(12);
        }
    }
    if (ix == 15) { STAMP_OUT(); return; }
    if (isO) __builtin_amdgcn_s_sleep(TAIL_SLEEP); else __builtin_amdgcn_s_sleep(FFN_SLEEP);
    constexpr unsigned L_W = 0, L_WD = 65536, L_ACT = 98304, L_GR = L_ACT + 8192, L_ST = L_GR + 2048;
    {
        const unsigned ch = (unsigned)((l & 7) ^ (((g & 1) | ((w & 1) << 1)) << 1));
        if (isO) {
            dma_paced(mksrd(wo, 4194304), (unsigned)(l >> 3) * 1024u + ch * 16, (unsigned)(512 * xc + 8 * w) * 1024u + ix * 128, 65536, L_W + w * 1024, 8192, 8);
        } else {
            const unsigned char* src = (const unsigned char*)(ch < 4 ? wg_ : wu_) + (size_t)(8 * w + (l >> 3)) * 3584 + fs * 64 + (ch & 3) * 16;
            _Pragma("unroll 1") for (int t = 0; t < 8; ++t) { dmag(src, L_W + (8 * t + w) * 1024); src += 64 * 3584; VMWAIT(FFN_DEPTH); }
            const v4u srd = mksrd(wd, 1835008);
            const unsigned sw = (unsigned)(((2 * w) & 3) << 2 | (w >> 1));
            dma_paced(srd, (l ^ sw) * 16, (unsigned)(32 * fs + 2 * w) * 1024u, 16384, L_WD + 2 * w * 1024, 16384, 2);
            dma_paced(srd, (l ^ sw ^ 4) * 16, (unsigned)(32 * fs + 2 * w + 1) * 1024u, 16384, L_WD + (2 * w + 1) * 1024, 16384, 2);
        }
        VMWAIT(0);
    }
    STAMP(7);
    BAR();
    v4u wf[16];
    {
        const unsigned a0 = L_W + (8 * g + trq) * 128 + (((isO ? 2 * (w & 3) + (trp >> 1) : (w & 3) + 4 * (trp >> 1)) ^ ((((trq >> 1) & 1) | ((g & 1) << 1)) << 1)) * 16) + 8 * (trp & 1);
#pragma unroll
        for (int s = 0; s < 16; ++s) wf[s] = ldstr2(a0 + s * 4096, a0 + s * 4096 + 512);
    }
    const v4u one = {0x3F803F80u, 0x3F803F80u, 0x3F803F80u, 0x3F803F80u};
    const v4u sel0 = selmask(l, 0) & one, sel1 = selmask(l, 8) & one;
    const int pb = (p16 >> 1) & 3, pk = 32 * (g >> 1) + 16 * (p16 >> 3) + 8 * (p16 & 1) + 4 * (g & 1);
    unsigned acta = L_ACT + pb * 1088 + (w * 64 + pk) * 2;
    PIN(acta);
    if (isO) {
        unsigned bfa = L_ACT + (l & 3) * 1088 + g * 16, ho = p16 < 4 ? W_HP + (unsigned)(xc * 4096 + (ix * 4 + w) * 128 + p16 * 32 + g * 8) : W_BYTES;
        PIN(bfa); PIN(ho);
        if (w == 7) { wait_flag<1>(rs, W_HINT + (unsigned)(wg - 128) * 128u + (l & 1) * 4, tag); __builtin_amdgcn_s_sleep(H4_SLEEP); }
        BAR();
        STAMP(8);
        v4u v[2];
        { const unsigned o[1] = {W_CTXP + (unsigned)(((4 * xc + (w >> 1)) * 4 + g) * 1024 + (w & 1) * 512 + p16 * 16)}; gather<2, 1>(rs, o, 256, v); }
        STAMP(9);
        f32x4 acc = {0.f, 0.f, 0.f, 0.f};
        acc = mfma16(sel0, v[0], acc); acc = mfma16(sel1, v[1], acc);
        sts64(acta, (v2u){pk2(acc.x, acc.y), pk2(acc.z, acc.w)});
        BAR();
        if (w < 4) {
            v4u bf[16];
#pragma unroll
            for (int s = 0; s < 8; ++s) { bf[s] = lds128(bfa + s * 64); SB(); }
            f32x4 d = {0.f, 0.f, 0.f, 0.f};
#pragma unroll
            for (int s = 0; s < 16; ++s) {
                d = mfma16(wf[s], bf[s], d);
                SB();
                if (s < 8) bf[s + 8] = lds128(bfa + (s + 8) * 64);
                SB();
            }
            gst64(rs, ho, (v2u){pk2(d.x, d.y), pk2(d.z, d.w)});
        } else {
            gst64(rs, threadIdx.x == 448 ? W_HINT + wg * 128 : W_BYTES, (v2u){tag, tag});
        }
        STAMP(10);
    } else {
        v4u df[4];
        {
            const unsigned r0 = 8 * g + trq;
#pragma unroll
            for (int k = 0; k < 4; ++k) {
                const unsigned ch = 8 * w + 4 * (k >> 1) + trp;
                const unsigned a0 = L_WD + r0 * 1024 + ((ch ^ ((trq << 2) | ((2 * g) & 3))) * 16) + 8 * (k & 1);
                df[k] = ldstr2(a0, (a0 ^ 16) + 4096);
            }
        }
        const v2u xr = *(const GAS v2u*)(x + pb * 512 + w * 64 + pk);
        f32x4 cs = {0.f, 0.f, 0.f, 0.f};
#pragma unroll
        for (int s = 0; s < 16; ++s) cs = mfma16(wf[s], one, cs);
        unsigned sta = L_ST + pb * 272 + (w * 4 + g) * 8, str = L_ST + (l & 3) * 272 + (l >> 2) * 16, bfa = L_ACT + (l & 3) * 1088 + g * 16;
        unsigned a2w = L_GR + (l & 3) * 80 + w * 16 + (g & 1) * 8 + (g >> 1) * 4, a2r = L_GR + (l & 3) * 80 + g * 16;
        unsigned oo = p16 < 4 ? W_OUTP + (unsigned)(fs * 4096 + w * 512 + (g >> 1) * 128 + p16 * 32 + (g & 1) * 16) : W_BYTES;
        f32x4 acc = {bflo(xr.x), bfhi(xr.x), bflo(xr.y), bfhi(xr.y)};
        PIN(cs); PIN(sta); PIN(str); PIN(bfa); PIN(a2w); PIN(a2r); PIN(oo); PIN(acc);
        if (w == 7) { wait_flag<1>(rs, W_HINT + (unsigned)(wg - 64) * 128u + (l & 1) * 4, tag); __builtin_amdgcn_s_sleep(H5_SLEEP); }
        BAR();
        STAMP(8);
        v4u v[4];
        {
            const unsigned off = W_HP + (unsigned)(g * 4096 + w * 512 + p16 * 16);
            const unsigned o[2] = {off, off + 16384};
            gather<4, 2>(rs, o, 256, v);
        }
        STAMP(9);
        acc = mfma16(sel0, v[0], acc); acc = mfma16(sel0, v[1], acc); acc = mfma16(sel1, v[2], acc); acc = mfma16(sel1, v[3], acc);
        sts64(acta, (v2u){pk2(acc.x, acc.y), pk2(acc.z, acc.w)});
        {
            float s1 = (acc.x + acc.y) + (acc.z + acc.w), s2 = acc.x * acc.x + acc.y * acc.y + acc.z * acc.z + acc.w * acc.w;
            s1 += dpp<0xB1>(s1); s2 += dpp<0xB1>(s2); s1 += dpp<0x128>(s1); s2 += dpp<0x128>(s2);
            sts64(sta, (v2u){__float_as_uint(s1), __float_as_uint(s2)});
        }
        BAR();
        if (w < 4) {
            v4u bf[16];
            bf[0] = lds128(bfa); SB();
            const f32x4 sv = __builtin_bit_cast(f32x4, lds128(str)); SB();
#pragma unroll
            for (int s = 1; s < 8; ++s) { bf[s] = lds128(bfa + s * 64); SB(); }
            f32x4 d = {0.f, 0.f, 0.f, 0.f};
            float s1, s2, mean, r2;
#pragma unroll
            for (int s = 0; s < 16; ++s) {
                d = mfma16(wf[s], bf[s], d);
                SB();
                if (s < 8) bf[s + 8] = lds128(bfa + (s + 8) * 64);
                if (s == 1) { s1 = sv.x + sv.z; s2 = sv.y + sv.w; }
                if (s == 2) { s1 += dpp<0x128>(s1); s2 += dpp<0x128>(s2); }
                if (s == 3) { s1 += dpp<0x124>(s1); s2 += dpp<0x124>(s2); }
                if (s == 5) s1 = xsum16(s1);
                if (s == 6) s2 = xsum16(s2);
                if (s == 8) s1 = xsum32(s1);
                if (s == 9) s2 = xsum32(s2);
                if (s == 10) { mean = s1 * (1.f / 512.f); s2 = s2 * (1.f / 512.f) - mean * mean; }
                if (s == 11) { r2 = __builtin_amdgcn_rcpf(max2(s2, 0.f) + 1e-5f); PIN(r2); }
                if (s == 12) { cs *= mean; PIN(cs); }
                SB();
            }
            const f32x4 y = d - cs;
            auto r0 = __builtin_amdgcn_permlane32_swap(__float_as_uint(y.x), __float_as_uint(y.z), false, false);
            auto r1 = __builtin_amdgcn_permlane32_swap(__float_as_uint(y.y), __float_as_uint(y.w), false, false);
            sts32(a2w, __uint_as_float(pk2(max2(__uint_as_float(r0[0]), 0.f) * (__uint_as_float(r0[1]) * r2), max2(__uint_as_float(r1[0]), 0.f) * (__uint_as_float(r1[1]) * r2))));
        } else {
            gst64(rs, threadIdx.x == 448 ? W_HINT + wg * 128 : W_BYTES, (v2u){tag, tag});
        }
        BAR();
        STAMP(10);
        {
            const v4u af = lds128(a2r);
            const f32x4 z = {0.f, 0.f, 0.f, 0.f};
            const f32x4 d0 = mfma16(df[0], af, z), d1 = mfma16(df[1], af, z), d2 = mfma16(df[2], af, z), d3 = mfma16(df[3], af, z);
            SB();
            const v4u o0 = {pk2(d0.x, d0.y), pk2(d0.z, d0.w), pk2(d1.x, d1.y), pk2(d1.z, d1.w)}, o1 = {pk2(d2.x, d2.y), pk2(d2.z, d2.w), pk2(d3.x, d3.y), pk2(d3.z, d3.w)};
            gst128(rs, oo, o0);
            gst128(rs, oo + 256, o1);
        }
        STAMP(11);
    }
    STAMP_OUT();
}

__global__ void __launch_bounds__(64) k_spin(const unsigned* st) {
    unsigned t0 = st[0]; int mn = 0, mx = 0;
    for (int i = threadIdx.x; i < 2048; i += 64) { mn = min(mn, (int)(st[i * 16] - t0)); if (i >= 0 * 8 && i < 128 * 8 && st[i * 16 + 12]) mx = max(mx, (int)(st[i * 16 + 12] - t0)); }
    for (int o = 32; o; o >>= 1) { mn = min(mn, __shfl_xor(mn, o)); mx = max(mx, __shfl_xor(mx, o)); }
    int m = mx - mn;
    if (m > 5000) m = 5000;
    unsigned long long a = __builtin_amdgcn_s_memrealtime();
    while (__builtin_amdgcn_s_memrealtime() < a + (unsigned long long)m * 20) __builtin_amdgcn_s_sleep(2);
}
extern "C" void kernel_launch(void* const* d_in, const int* in_sizes, int n_in, void* d_out, int out_size, void* d_ws, size_t ws_size,
                              hipStream_t stream) {
    (void)in_sizes; (void)n_in; (void)out_size;
    unsigned char* arena = (unsigned char*)d_ws + ((ws_size - W_BYTES) & ~(size_t)4095);
    (void)hipFuncSetAttribute((const void*)fused_decode, hipFuncAttributeMaxDynamicSharedMemorySize, LDS_BYTES);
    fused_decode<<<256, 512, LDS_BYTES, stream>>>((const u16*)d_in[2], (const u16*)d_in[3], (const u16*)d_in[1], (const u16*)d_in[0], arena, (const u16*)d_in[4],
                                                  (const u16*)d_in[5], (const u16*)d_in[7], (const u16*)d_in[8], (const u16*)d_in[9], (u16*)d_out);
    k_spin<<<1, 64, 0, stream>>>((const unsigned*)(arena + W_STAMP));
}
```

```cpp
#define KV_DEPTH "40"
#include <hip/hip_runtime.h>
#include <stdint.h>

#define GAS __attribute__((address_space(1)))
#define LAS __attribute__((address_space(3)))
typedef unsigned short u16;
typedef unsigned v4u __attribute__((ext_vector_type(4)));
typedef unsigned v2u __attribute__((ext_vector_type(2)));
typedef float f32x4 __attribute__((ext_vector_type(4)));
typedef float f32x2 __attribute__((ext_vector_type(2)));
typedef float f32x16 __attribute__((ext_vector_type(16)));
typedef short s16x4 __attribute__((ext_vector_type(4)));
typedef __bf16 bf16x8 __attribute__((ext_vector_type(8)));
typedef __bf16 bf16x2 __attribute__((ext_vector_type(2)));

#define KNOB(n, v)
#define NT_POLICY " nt"
#define GU_POLICY ""
#define HOP1_SLEEP 12
#define UK_SLEEP "16"
#define KV_EARLY 1
#define KV_EARLY_AT 4
#define STAT_SLEEP 6
#define RETRY_SLEEP 1
#define PZ_SLEEP 0
#define PART_SLEEP2 20
#define STAT_GATE 0
#define QZ_SLEEP 14
#define PZC_SLEEP 4
#define AG_SLEEP 40
#define D2 125
#define D3 180
#define D4 370
#define D5 460
#define D6 610
#define KVPF 0
#define KVPF_SLEEP 0
#define PZ_AT 1
#define WO_DEPTH "3"
#define FFN_DEPTH 2
#define Q_FIRST 0
#define Q_SLEEP 4
#define TAIL_SLEEP 55
#define FFN_SLEEP 40
#define PART_SLEEP 2
#define H4_SLEEP 6
#define H5_SLEEP 4
#define H6_SLEEP 8
#define CODE_BYTES "17300"

constexpr unsigned W_EPOCH = 0;
constexpr unsigned W_HINT = 4096;
constexpr unsigned W_QP = W_HINT + 32768;
constexpr unsigned W_QABS = W_QP + 262144;
constexpr unsigned W_STAT = W_QABS + 196608;
constexpr unsigned W_CTXP = W_STAT + 65536;
constexpr unsigned W_OUTP = W_CTXP + 131072;
constexpr unsigned W_HP = W_OUTP + 56 * 4096;
constexpr unsigned W_PZF = W_OUTP + 64 * 4096;
constexpr unsigned W_QZF = W_PZF + 4096;
constexpr unsigned W_TIME = W_QZF + 4096;
constexpr unsigned W_PART = 1u << 20;
constexpr unsigned W_STAMP = W_PART + (4u << 20);
constexpr unsigned W_BYTES = 6u << 20;
static_assert(W_TIME + 16384 <= W_PART, "arena");
constexpr int LDS_BYTES = 163840;
constexpr unsigned POISON = 0xFFFFFFFFu;
constexpr int SPIN_CAP = 1 << 16;

__device__ __forceinline__ unsigned pk2(float a, float b) { f32x2 v = {a, b}; return __builtin_bit_cast(unsigned, __builtin_convertvector(v, bf16x2)); }
__device__ __forceinline__ float bflo(unsigned u) { return __uint_as_float(u << 16); }
__device__ __forceinline__ float bfhi(unsigned u) { return __uint_as_float(u & 0xFFFF0000u); }
__device__ __forceinline__ f32x4 mfma16(v4u a, v4u b, f32x4 c) { return __builtin_amdgcn_mfma_f32_16x16x32_bf16(__builtin_bit_cast(bf16x8, a), __builtin_bit_cast(bf16x8, b), c, 0, 0, 0); }
__device__ __forceinline__ f32x16 mfma32(v4u a, v4u b, f32x16 c) { return __builtin_amdgcn_mfma_f32_32x32x16_bf16(__builtin_bit_cast(bf16x8, a), __builtin_bit_cast(bf16x8, b), c, 0, 0, 0); }
__device__ __forceinline__ v4u lds128(unsigned a) { return *(LAS v4u*)(uintptr_t)a; }
__device__ __forceinline__ v2u ldstr(unsigned a) { return __builtin_bit_cast(v2u, __builtin_amdgcn_ds_read_tr16_b64_v4i16((LAS s16x4*)(uintptr_t)a)); }
__device__ __forceinline__ v4u ldstr2(unsigned a0, unsigned a1) { v2u x = ldstr(a0), y = ldstr(a1); return (v4u){x.x, x.y, y.x, y.y}; }
__device__ __forceinline__ void sts128(unsigned a, v4u v) { *(LAS v4u*)(uintptr_t)a = v; }
__device__ __forceinline__ void sts64(unsigned a, v2u v) { *(LAS v2u*)(uintptr_t)a = v; }
__device__ __forceinline__ void sts32(unsigned a, float v) { *(LAS float*)(uintptr_t)a = v; }
__device__ __forceinline__ float lds32(unsigned a) { return *(LAS float*)(uintptr_t)a; }
#define BAR() do { asm volatile("" ::: "memory"); __builtin_amdgcn_s_waitcnt(0xC07F); asm volatile("s_barrier" ::: "memory"); } while (0)
#define VMWAIT_(n) asm volatile("s_waitcnt vmcnt(" #n ")" ::: "memory")
#define VMWAIT(n) VMWAIT_(n)
#define CFENCE() asm volatile("" ::: "memory")
typedef __amdgpu_buffer_rsrc_t rsrc_t;
__device__ __forceinline__ v4u gld128(rsrc_t r, unsigned off, unsigned so = 0) { return __builtin_amdgcn_raw_buffer_load_b128(r, off, so, 16); }
__device__ __forceinline__ v2u gld64(rsrc_t r, unsigned off, unsigned so = 0) { return __builtin_amdgcn_raw_buffer_load_b64(r, off, so, 16); }
__device__ __forceinline__ void gst128(rsrc_t r, unsigned off, v4u v, unsigned so = 0) { __builtin_amdgcn_raw_buffer_store_b128(v, r, off, so, 16); }
__device__ __forceinline__ void gst64(rsrc_t r, unsigned off, v2u v, unsigned so = 0) { __builtin_amdgcn_raw_buffer_store_b64(v, r, off, so, 16); }
__device__ __forceinline__ void gst32(rsrc_t r, unsigned off, unsigned v) { __builtin_amdgcn_raw_buffer_store_b32(v, r, off, 0, 16); }
__device__ __forceinline__ v4u mksrd(const void* p, unsigned bytes) {
    const uint64_t a = (uint64_t)p;
    return (v4u){(unsigned)__builtin_amdgcn_readfirstlane((int)(unsigned)a), (unsigned)__builtin_amdgcn_readfirstlane((int)(unsigned)(a >> 32)) & 0xFFFFu, bytes, 0x00020000u};
}
__device__ __forceinline__ void dma(v4u srd, unsigned voff, unsigned soff, unsigned dst) {
    asm volatile("s_mov_b32 m0, %3\n\ts_nop 0\n\tbuffer_load_dwordx4 %0, %1, %2 offen" NT_POLICY " lds" :: "v"(voff), "s"(srd), "s"(soff), "s"(dst) : "memory");
}
__device__ __forceinline__ void dmag(const void* p, unsigned dst) {
    asm volatile("s_mov_b32 m0, %1\n\ts_nop 0\n\tglobal_load_lds_dwordx4 %0, off" GU_POLICY :: "v"(p), "s"(dst) : "memory");
}
__device__ __forceinline__ void dma_loop(v4u srd, unsigned voff, unsigned soff, unsigned sstep, unsigned dst, unsigned dstep, unsigned n) {
    asm volatile("s_mov_b32 m0, %4\n\ts_nop 3\n"
                 ".Ldl%=:\n\t"
                 "buffer_load_dwordx4 %2, %3, %0 offen" NT_POLICY " lds\n\t"
                 "s_add_u32 m0, m0, %6\n\ts_add_u32 %0, %0, %5\n\ts_sub_u32 %1, %1, 1\n\ts_cmp_lg_u32 %1, 0\n\ts_cbranch_scc1 .Ldl%="
                 : "+s"(soff), "+s"(n) : "v"(voff), "s"(srd), "s"(dst), "s"(sstep), "s"(dstep) : "scc", "memory");
}
__device__ __forceinline__ void dma_paced(v4u srd, unsigned voff, unsigned soff, unsigned sstep, unsigned dst, unsigned dstep, unsigned n) {
    asm volatile("s_mov_b32 m0, %4\n\ts_nop 3\n"
                 ".Ldp%=:\n\t"
                 "buffer_load_dwordx4 %2, %3, %0 offen" NT_POLICY " lds\n\t"
                 "s_waitcnt vmcnt(" WO_DEPTH ")\n\t"
                 "s_add_u32 m0, m0, %6\n\ts_add_u32 %0, %0, %5\n\ts_sub_u32 %1, %1, 1\n\ts_cmp_lg_u32 %1, 0\n\ts_cbranch_scc1 .Ldp%="
                 : "+s"(soff), "+s"(n) : "v"(voff), "s"(srd), "s"(dst), "s"(sstep), "s"(dstep) : "scc", "memory");
}
__device__ __forceinline__ float max3(float a, float b, float c) { float r; asm("v_max3_f32 %0, %1, %2, %3" : "=v"(r) : "v"(a), "v"(b), "v"(c)); return r; }
__device__ __forceinline__ float max2(float a, float b) { float r; asm("v_max_f32 %0, %1, %2" : "=v"(r) : "v"(a), "v"(b)); return r; }
__device__ __forceinline__ unsigned umax4(v4u v, unsigned m) { return max(max(max(v.x, v.y), max(v.z, v.w)), m); }
__device__ __forceinline__ float xsum16(float v) { auto r = __builtin_amdgcn_permlane16_swap(__float_as_uint(v), __float_as_uint(v), false, false); return __uint_as_float(r[0]) + __uint_as_float(r[1]); }
__device__ __forceinline__ float xsum32(float v) { auto r = __builtin_amdgcn_permlane32_swap(__float_as_uint(v), __float_as_uint(v), false, false); return __uint_as_float(r[0]) + __uint_as_float(r[1]); }
__device__ __forceinline__ float xmax16(float v) { auto r = __builtin_amdgcn_permlane16_swap(__float_as_uint(v), __float_as_uint(v), false, false); return fmaxf(__uint_as_float(r[0]), __uint_as_float(r[1])); }
__device__ __forceinline__ float xmax32(float v) { auto r = __builtin_amdgcn_permlane32_swap(__float_as_uint(v), __float_as_uint(v), false, false); return fmaxf(__uint_as_float(r[0]), __uint_as_float(r[1])); }
template <int CTRL> __device__ __forceinline__ float dpp(float v) { return __int_as_float(__builtin_amdgcn_update_dpp(0, __float_as_int(v), CTRL, 0xf, 0xf, true)); }
__device__ __forceinline__ float rsum16(float v) { v += dpp<0xB1>(v); v += dpp<0x4E>(v); v += dpp<0x141>(v); v += dpp<0x140>(v); return v; }
__device__ __forceinline__ float rmax16(float v) { v = fmaxf(v, dpp<0xB1>(v)); v = fmaxf(v, dpp<0x4E>(v)); v = fmaxf(v, dpp<0x141>(v)); v = fmaxf(v, dpp<0x140>(v)); return v; }
__device__ __forceinline__ float wsum(float v) { return xsum32(xsum16(rsum16(v))); }

#define STAMP(k) do {} while (0)
#define STAMP_OUT() do {} while (0)

__device__ __forceinline__ v4u selmask(int l, int r0) {
    const int j = (l & 15) - r0;
    const unsigned m = (j & 1) ? 0xFFFF0000u : 0x0000FFFFu;
    return (v4u){(j >> 1) == 0 ? m : 0u, (j >> 1) == 1 ? m : 0u, (j >> 1) == 2 ? m : 0u, (j >> 1) == 3 ? m : 0u};
}

__device__ __forceinline__ unsigned gld32(rsrc_t r, unsigned off) { return __builtin_amdgcn_raw_buffer_load_b32(r, off, 0, 16); }
template <int SLEEP> __device__ __forceinline__ void wait_flag(rsrc_t rs, unsigned off, unsigned tag) {
    _Pragma("unroll 1") for (int it = 0; it < SPIN_CAP; ++it) {
        CFENCE();
        const unsigned v = gld32(rs, off);
        if (__all(v == tag)) break;
        __builtin_amdgcn_s_sleep(SLEEP);
    }
    CFENCE();
}
__device__ __forceinline__ unsigned now16() { return (unsigned)__builtin_amdgcn_s_memrealtime() & 0xFFFFu; }
__device__ __forceinline__ void wait_until(unsigned t) {
    _Pragma("unroll 1") for (int it = 0; it < 512; ++it) { if ((short)(now16() - t) >= 0) break; }
}
#define SB() __builtin_amdgcn_sched_barrier(0)
#define PIN(x) asm volatile("" : "+v"(x))
template <int N, int NO> __device__ __forceinline__ void gissue(rsrc_t rs, const unsigned (&off)[NO], unsigned stride, v4u (&v)[N]) {
#pragma unroll
    for (int k = 0; k < N; ++k) v[k] = gld128(rs, off[k % NO], (k / NO) * stride);
}
template <int N, int NO> __device__ __forceinline__ void gcheck(rsrc_t rs, const unsigned (&off)[NO], unsigned stride, v4u (&v)[N]) {
    unsigned m = 0;
#pragma unroll
    for (int k = 0; k < N; ++k) m = umax4(v[k], m);
    if (__builtin_expect(!__all(m != POISON), 0)) {
        _Pragma("unroll 1") for (int it = 0; it < SPIN_CAP; ++it) {
            CFENCE();
            if (m == POISON) { gissue<N, NO>(rs, off, stride, v); m = 0;
#pragma unroll
                for (int k = 0; k < N; ++k) m = umax4(v[k], m); }
            if (__all(m != POISON)) break;
        }
    }
    CFENCE();
}
template <int N, int NO> __device__ __forceinline__ void gather(rsrc_t rs, const unsigned (&off)[NO], unsigned stride, v4u (&v)[N]) {
    CFENCE();
    gissue<N, NO>(rs, off, stride, v);
    gcheck<N, NO>(rs, off, stride, v);
}

__global__ void __launch_bounds__(512, 2)
fused_decode(const u16* __restrict__ wq, const u16* __restrict__ wuk, const u16* __restrict__ ckv_arg, const u16* __restrict__ x,
             unsigned char* __restrict__ ws_arg, const u16* __restrict__ wuv, const u16* __restrict__ wo, const u16* __restrict__ wg_,
             const u16* __restrict__ wu_, const u16* __restrict__ wd, u16* __restrict__ out) {
    extern __shared__ __attribute__((aligned(16))) unsigned char lds_[];
    STAMP(0);
    v4u xv;
    unsigned rawtag;
    unsigned char* ws;
    const u16* ckv;
    {
        unsigned t0, t1, t2, t3, pfs;
#define PDMA(V) "buffer_load_dwordx4 " V ", s[52:55], s58 offen" NT_POLICY " lds\n\t"
#define PSTEP "s_add_u32 m0, m0, 0x400\n\ts_add_u32 s58, s58, 0x1000\n\t"
        asm volatile(
            "s_load_dwordx8 s[40:47], %[ka], 0x0\n\t"
            "s_load_dwordx2 %[ws], %[ka], 0x20\n\t"
            "v_readfirstlane_b32 s50, %[tid]\n\t"
            "s_bfe_u32 s51, %[wg], 0x40003\n\t"
            "s_lshr_b32 s50, s50, 6\n\t"
            "s_cmp_lt_u32 s50, 4\n\t"
            "s_cbranch_scc1 .Ldm%=\n\t"
            "s_cmp_lt_u32 %[wg], 0x80\n\t"
            "s_cbranch_scc1 .Lxl%=\n\t"
            "s_getpc_b64 s[56:57]\n\t"
            "s_lshl_b32 s60, s51, 2\n\t"
            "s_add_u32 s60, s60, s50\n\t"
            "s_mul_i32 s60, s60, 320\n\t"
            "s_sub_u32 s60, s60, 1280\n\t"
            "s_min_u32 s60, s60, " CODE_BYTES "\n\t"
            "s_add_u32 s56, s56, s60\n\t"
            "s_addc_u32 s57, s57, 0\n\t"
            "s_load_dword %[pf], s[56:57], 0x0\n\t"
            "s_load_dword %[pf], s[56:57], 0x40\n\t"
            "s_load_dword %[pf], s[56:57], 0x80\n\t"
            "s_load_dword %[pf], s[56:57], 0xc0\n\t"
            "s_load_dword %[pf], s[56:57], 0x100\n\t"
            "s_waitcnt lgkmcnt(0)\n\t"
            "s_load_dword %[tag], %[ws], 0x0\n\t"
            "s_branch .Lpe%=\n"
            ".Lxl%=:\n\t"
            "v_lshlrev_b32 %[t1], 4, %[tid]\n\t"
            "s_waitcnt lgkmcnt(0)\n\t"
            "global_load_dwordx4 %[xv], %[t1], s[46:47] offset:-4096\n\t"
            "s_load_dword %[tag], %[ws], 0x0\n\t"
            "s_branch .Lpe%=\n"
            ".Ldm%=:\n\t"
            "v_bfe_u32 %[t2], %[tid], 4, 2\n\t"
            "v_and_b32 %[t3], 15, %[tid]\n\t"
            "s_and_b32 s59, %[wg], 7\n\t"
            "s_lshl_b32 s62, s50, 15\n\t"
            "s_lshl_b32 s58, s59, 10\n\t"
            "s_lshr_b32 s60, s51, 2\n\t"
            "s_lshl_b32 s60, s60, 20\n\t"
            "s_and_b32 s61, s51, 3\n\t"
            "s_lshl_b32 s61, s61, 8\n\t"
            "s_add_u32 s58, s58, s62\n\t"
            "s_add_u32 s60, s60, s61\n\t"
            "s_add_u32 s58, s58, s60\n\t"
            "v_lshlrev_b32 %[t0], 2, %[t2]\n\t"
            "v_xor_b32 %[t1], s50, %[t3]\n\t"
            "v_xor_b32 %[t1], %[t0], %[t1]\n\t"
            "v_lshlrev_b32 %[t0], 13, %[t2]\n\t"
            "v_lshl_add_u32 %[t0], %[t1], 4, %[t0]\n\t"
            "s_lshl_b32 s60, s50, 10\n\t"
            "s_add_u32 m0, s60, 0x20000\n\t"
            "s_mov_b32 s54, 0x400000\n\t"
            "s_mov_b32 s55, 0x20000\n\t"
            "s_mov_b32 s61, 8\n\t"
            "s_cmp_lt_u32 %[wg], 0x80\n\t"
            "s_waitcnt lgkmcnt(0)\n\t"
            "s_cselect_b32 s52, s40, s42\n\t"
            "s_cselect_b32 s53, s41, s43\n\t"
            "s_and_b32 s53, s53, 0xffff\n\t"
            "s_mov_b64 %[kv], s[44:45]\n\t"
            "s_load_dword %[tag], %[ws], 0x0\n\t"
            "s_cmp_lt_u32 %[wg], 0x80\n\t"
            "s_cbranch_scc1 .Lf1%=\n\t"
            "s_cmp_lg_u32 s50, 0\n\t"
            "s_cbranch_scc1 .Lf0%=\n\t"
            "s_lshl_b32 s60, s59, 12\n\t"
            "s_and_b32 s62, s51, 3\n\t"
            "s_lshl_b32 s62, s62, 10\n\t"
            "s_add_u32 s60, s60, s62\n\t"
            "s_lshr_b32 s62, s51, 2\n\t"
            "s_lshl_b32 s62, s62, 8\n\t"
            "s_add_u32 s60, s60, s62\n\t"
            "s_add_u32 s60, s60, %[oq]\n\t"
            "v_lshlrev_b32 %[t2], 15, %[t2]\n\t"
            "v_lshl_add_u32 %[t2], %[t3], 4, %[t2]\n\t"
            "v_add_u32 %[t2], s60, %[t2]\n\t"
            "global_store_dwordx4 %[t2], %[pz], %[ws] sc1\n\t"
            "s_lshl_b32 s60, %[wg], 2\n\t"
            "s_add_u32 s60, s60, %[oz]\n\t"
            "v_mov_b32 %[t2], s60\n\t"
            "s_waitcnt vmcnt(0) lgkmcnt(0)\n\t"
            "s_add_u32 s60, %[tag], 0x01010101\n\t"
            "v_mov_b32 %[t3], s60\n\t"
            "s_mov_b64 exec, 1\n\t"
            "global_store_dword %[t2], %[t3], %[ws] sc1\n\t"
            "s_mov_b64 exec, -1\n\t"
            "s_branch .Lf1%=\n"
            ".Lf0%=:\n\t"
            "s_sleep " UK_SLEEP "\n"
            ".Lf1%=:\n\t"
            PDMA("%[t0]")
            "s_add_u32 m0, m0, 0x1000\n\ts_add_u32 s58, s58, 0x20000\n\ts_sub_u32 s61, s61, 1\n\ts_cmp_lg_u32 s61, 0\n\ts_cbranch_scc1 .Lf1%=\n"
            ".Lpe%=:"
            : [xv] "=&v"(xv), [tag] "=&s"(rawtag), [ws] "=&s"(ws), [kv] "=&s"(ckv), [pf] "=&s"(pfs), [t0] "=&v"(t0), [t1] "=&v"(t1), [t2] "=&v"(t2), [t3] "=&v"(t3)
            : [ka] "s"(__builtin_amdgcn_kernarg_segment_ptr()), [tid] "v"(threadIdx.x), [wg] "s"(blockIdx.x), [pz] "v"((v4u){POISON, POISON, POISON, POISON}),
              [oq] "i"(W_QABS), [oz] "i"(W_QZF - 512)
            : "s40", "s41", "s42", "s43", "s44", "s45", "s46", "s47", "s50", "s51", "s52", "s53", "s54", "s55", "s56", "s57", "s58", "s59", "s60", "s61", "s62", "scc", "memory");
    }
    SB();
    const int wg = blockIdx.x, l = threadIdx.x & 63, w = __builtin_amdgcn_readfirstlane(threadIdx.x >> 6);
    const int xc = wg & 7, ix = (wg >> 3) & 15;
    const int g = l >> 4, p16 = l & 15;
    const int hh = 4 * xc + (ix & 3), qd = ix >> 2;
    const int ab = xc >> 1, arow = xc * 16 + ix;
    unsigned tag;
#define GET_TAG() asm volatile("s_waitcnt lgkmcnt(0)\n\ts_add_u32 %0, %1, 0x01010101" : "=s"(tag) : "s"(rawtag) : "scc", "memory")
    const rsrc_t rs = __builtin_amdgcn_make_buffer_rsrc(ws, 0, W_BYTES, 0x00020000);
    const unsigned trq = (unsigned)(p16 >> 2), trp = (unsigned)(l & 3);
    const unsigned tra = (8 * g + trq) * 256 + (((2 * w + (trp >> 1)) ^ ((trq << 2) | ((2 * g) & 3))) * 16) + 8 * (trp & 1);

    auto hint = [&]() {
        v4u h;
        _Pragma("unroll 1") for (int it = 0; it < SPIN_CAP; ++it) {
            CFENCE();
            h = gld128(rs, W_HINT + (unsigned)wg * 128u);
            if (__all(h.y == tag && h.z == tag && h.w == tag)) break;
            __builtin_amdgcn_s_sleep(3);
        }
        CFENCE();
        return (unsigned)__builtin_amdgcn_readfirstlane((int)h.x);
    };

    if (__builtin_expect(wg < 128, 1)) {
        constexpr unsigned L_Q = 131072, L_XN = 98304, L_MX = 0, L_P = 1024, L_LS = 9216, L_CL = 12288, L_WUV = 32768;
        auto kvload = [&](unsigned cb, unsigned n) {
            const unsigned v0 = (unsigned)g * 1024u + (unsigned)((p16 ^ (2 * g)) * 16);
            unsigned so = (unsigned)(arow * 128 + 32 * w) * 1024u + cb * 256;
#define KDMA(V) "buffer_load_dwordx4 " V ", %6, %0 offen" NT_POLICY " lds\n\t"
#define KSTEP "s_add_u32 m0, m0, 0x400\n\ts_add_u32 %0, %0, 0x1000\n\t"
            asm volatile("s_mov_b32 m0, %7\n\ts_nop 3\n"
                         ".Lkv%=:\n\t"
                         KDMA("%2") KSTEP KDMA("%3") KSTEP KDMA("%4") KSTEP KDMA("%5") KSTEP KDMA("%2") KSTEP KDMA("%3") KSTEP KDMA("%4") KSTEP KDMA("%5")
                         "s_add_u32 m0, m0, 25600\n\ts_add_u32 %0, %0, 0xffff9100\n\ts_sub_u32 %1, %1, 1\n\ts_cmp_lg_u32 %1, 0\n\ts_cbranch_scc1 .Lkv%="
                         : "+s"(so), "+s"(n) : "v"(v0), "v"(v0 ^ 128u), "v"(v0 ^ 16u), "v"(v0 ^ 144u), "s"(mksrd(ckv, 16777216u)), "s"(cb * 32768 + w * 8192) : "scc", "memory");
        };
        auto kvpaced = [&](unsigned cb, unsigned n) {
            const unsigned v0 = (unsigned)g * 1024u + (unsigned)((p16 ^ (2 * g)) * 16);
            unsigned so = (unsigned)(arow * 128 + 32 * w) * 1024u + cb * 256;
#define KDMP(V) "buffer_load_dwordx4 " V ", %6, %0 offen" NT_POLICY " lds\n\ts_waitcnt vmcnt(" KV_DEPTH ")\n\t"
            asm volatile("s_mov_b32 m0, %7\n\ts_nop 3\n"
                         ".Lkp%=:\n\t"
                         KDMP("%2") KSTEP KDMP("%3") KSTEP KDMP("%4") KSTEP KDMP("%5") KSTEP KDMP("%2") KSTEP KDMP("%3") KSTEP KDMP("%4") KSTEP KDMP("%5")
                         "s_add_u32 m0, m0, 25600\n\ts_add_u32 %0, %0, 0xffff9100\n\ts_sub_u32 %1, %1, 1\n\ts_cmp_lg_u32 %1, 0\n\ts_cbranch_scc1 .Lkp%="
                         : "+s"(so), "+s"(n) : "v"(v0), "v"(v0 ^ 128u), "v"(v0 ^ 16u), "v"(v0 ^ 144u), "s"(mksrd(ckv, 16777216u)), "s"(cb * 32768 + w * 8192) : "scc", "memory");
        };
        if (w < 4) {
            GET_TAG();
            STAMP(1);
            VMWAIT(KV_EARLY_AT);
            kvload(0, 1);
            VMWAIT(8);
        } else {
            GET_TAG();
            STAMP(1);
            asm volatile("s_waitcnt vmcnt(0)" : "+v"(xv) :: "memory");
            float v[8] = {bflo(xv.x), bfhi(xv.x), bflo(xv.y), bfhi(xv.y), bflo(xv.z), bfhi(xv.z), bflo(xv.w), bfhi(xv.w)};
            const float mean = wsum(((v[0] + v[1]) + (v[2] + v[3])) + ((v[4] + v[5]) + (v[6] + v[7]))) * (1.f / 512.f);
            float ss = 0.f;
#pragma unroll
            for (int j = 0; j < 8; ++j) { v[j] -= mean; ss += v[j] * v[j]; }
            const float rstd = rsqrtf(wsum(ss) * (1.f / 512.f) + 1e-5f);
            if (g == qd) sts128(L_XN + (w - 4) * 320 + p16 * 16, (v4u){pk2(v[0] * rstd, v[1] * rstd), pk2(v[2] * rstd, v[3] * rstd), pk2(v[4] * rstd, v[5] * rstd), pk2(v[6] * rstd, v[7] * rstd)});
        }
        unsigned qpo = p16 < 4 ? W_QP + (unsigned)(((hh * 4 + qd) * 8 + w) * 256 + p16 * 64 + g * 16) : W_BYTES, qa0 = L_Q + tra, qa1 = L_Q + (tra ^ 16) + 1024, xna = L_XN + (l & 3) * 320 + g * 16;
        PIN(qpo); PIN(qa0); PIN(qa1); PIN(xna);
        STAMP(12);
        BAR();
        {
            v4u af[4], bf[4];
#pragma unroll
            for (int s = 0; s < 4; ++s) { af[s] = ldstr2(qa0 + s * 8192, qa1 + s * 8192); bf[s] = lds128(xna + s * 64); SB(); }
            SB();
            f32x4 acc = {0.f, 0.f, 0.f, 0.f};
#pragma unroll
            for (int s = 0; s < 4; ++s) acc = mfma16(af[s], bf[s], acc);
            gst128(rs, qpo, (v4u){pk2(acc.x, acc.y), tag, pk2(acc.z, acc.w), tag});
        }
        STAMP(11);
        BAR();
        if (w < 4) {
            kvpaced(KV_EARLY, 4 - KV_EARLY);
            const int r = l & 31, hf = l >> 5;
            unsigned fa[8];
#pragma unroll
            for (int s = 0; s < 8; ++s) fa[s] = (unsigned)(r * 256 + (((2 * s + hf) ^ (((r & 7) << 1) | ((r >> 3) & 1))) * 16)) + w * 8192;
            v4u kf[32];
            VMWAIT(16);
#pragma unroll
            for (int s = 0; s < 16; ++s) kf[s] = lds128(fa[s & 7] + (s >> 3) * 32768);
#pragma unroll
            for (int s = 0; s < 8; ++s) { fa[s] += 65536; PIN(fa[s]); }
            BAR();
            STAMP(2);
            VMWAIT(8);
#pragma unroll
            for (int s = 16; s < 24; ++s) kf[s] = lds128(fa[s & 7]);
            VMWAIT(0);
            STAMP(3);
#pragma unroll
            for (int s = 24; s < 32; ++s) kf[s] = lds128(fa[s & 7] + 32768);
            unsigned pa = L_P + (unsigned)((w * 2 + (r >> 4)) * 1024 + hf * 256 + (r & 15) * 16), mxw = L_MX + r * 16 + w * 4, mxr = L_MX + r * 16;
            unsigned lsw = L_LS + r * 32 + hf * 16 + w * 4;
#pragma unroll
            for (int s = 0; s < 8; ++s) { fa[s] += L_Q - 65536 - w * 8192; PIN(fa[s]); }
            PIN(pa); PIN(mxw); PIN(mxr); PIN(lsw);
            __builtin_amdgcn_s_setprio(2);
            BAR();
            STAMP(4);
            f32x16 sa;
#pragma unroll
            for (int j = 0; j < 16; ++j) sa[j] = 0.f;
            {
                constexpr int QD = 6;
                v4u qf[32];
#pragma unroll
                for (int s = 0; s < QD; ++s) { qf[s] = lds128(fa[s & 7] + (s >> 3) * 8192); SB(); }
#pragma unroll
                for (int s = 0; s < 32; ++s) {
                    sa = mfma32(kf[s], qf[s], sa);
                    SB();
                    if (s + QD < 32) qf[s + QD] = lds128(fa[(s + QD) & 7] + ((s + QD) >> 3) * 8192);
                    SB();
                }
            }
            float mw;
            {
                float m, t1, t2;
                asm volatile("s_nop 11\n\tv_max3_f32 %0, %3, %4, %5\n\tv_max3_f32 %1, %6, %7, %8\n\tv_max3_f32 %2, %9, %10, %11\n\t"
                             "v_max3_f32 %0, %0, %12, %13\n\tv_max3_f32 %1, %1, %14, %15\n\tv_max3_f32 %2, %2, %16, %17\n\tv_max3_f32 %0, %0, %1, %18\n\tv_max_f32 %0, %0, %2"
                             : "=&v"(m), "=&v"(t1), "=&v"(t2)
                             : "v"(sa[0]), "v"(sa[1]), "v"(sa[2]), "v"(sa[3]), "v"(sa[4]), "v"(sa[5]), "v"(sa[6]), "v"(sa[7]), "v"(sa[8]), "v"(sa[9]), "v"(sa[10]), "v"(sa[11]),
                               "v"(sa[12]), "v"(sa[13]), "v"(sa[14]), "v"(sa[15]));
                auto x = __builtin_amdgcn_permlane32_swap(__float_as_uint(m), __float_as_uint(m), false, false);
                mw = max2(__uint_as_float(x[0]), __uint_as_float(x[1]));
            }
            sts32(mxw, mw);
            f32x2 e[8];
#pragma unroll
            for (int j = 0; j < 8; ++j) e[j] = (f32x2){sa[2 * j] - mw, sa[2 * j + 1] - mw};
#pragma unroll
            for (int j = 0; j < 8; ++j) e[j] = (f32x2){__builtin_amdgcn_exp2f(e[j].x), __builtin_amdgcn_exp2f(e[j].y)};
            {
                const f32x2 t = ((e[0] + e[1]) + (e[2] + e[3])) + ((e[4] + e[5]) + (e[6] + e[7]));
                sts32(lsw, t.x + t.y);
            }
            STAMP(5);
            BAR();
            const f32x4 mv = __builtin_bit_cast(f32x4, lds128(mxr));
            const float f = __builtin_amdgcn_exp2f(mw - max2(max3(mv.x, mv.y, mv.z), mv.w));
#pragma unroll
            for (int j = 0; j < 4; ++j) e[j] *= f;
            sts128(pa, (v4u){pk2(e[0].x, e[0].y), pk2(e[1].x, e[1].y), pk2(e[2].x, e[2].y), pk2(e[3].x, e[3].y)});
#pragma unroll
            for (int j = 4; j < 8; ++j) e[j] *= f;
            sts128(pa + 512, (v4u){pk2(e[4].x, e[4].y), pk2(e[5].x, e[5].y), pk2(e[6].x, e[6].y), pk2(e[7].x, e[7].y)});
            BAR();
            __builtin_amdgcn_s_setprio(0);
            STAMP(6);
        } else {
            const int ow = w - 4;
            const unsigned va = (unsigned)(ow * 32768 + (16 * (g >> 1) + 4 * (g & 1) + trq) * 256 + (((trp >> 1) ^ (8 * (g & 1) + 2 * trq)) * 16) + 8 * (trp & 1));
            v4u vf[4][8];
            auto vread = [&]() {
#pragma unroll
                for (int kg = 0; kg < 4; ++kg)
#pragma unroll
                    for (int ct = 0; ct < 8; ++ct) vf[kg][ct] = ldstr2((va ^ (ct * 32)) + kg * 8192, (va ^ (ct * 32 + 16)) + kg * 8192 + 2048);
            };
            {
                const unsigned off[1] = {W_QABS + (unsigned)((ab * 32 + 8 * ow) * 1024 + l * 16)};
                const unsigned qa = L_Q + (unsigned)(g * 8192 + 8 * ow * 256 + ((p16 ^ (ow & 1)) * 16));
                if (ow == 3) {
                    __builtin_amdgcn_s_setprio(3);
                    __builtin_amdgcn_s_sleep(QZ_SLEEP);
                    _Pragma("unroll 1") for (int it = 0; it < SPIN_CAP; ++it) {
                        CFENCE();
                        const v2u z = gld64(rs, W_QZF + l * 8);
                        if (__all(z.x == tag && z.y == tag)) break;
                        __builtin_amdgcn_s_sleep(8);
                    }
                    __builtin_amdgcn_s_setprio(0);
                    STAMP(13);
                }
                BAR();
                STAMP(2);
                __builtin_amdgcn_s_sleep(Q_SLEEP);
                _Pragma("unroll 1") for (int it = 0;; ++it) {
                    CFENCE();
                    v4u qv[8];
#pragma unroll
                    for (int k = 0; k < 8; ++k) { qv[k] = gld128(rs, off[0], k * 1024); SB(); }
                    if (ow < 2 && it == 0) vread();
                    unsigned m = 0;
#pragma unroll
                    for (int k = 0; k < 8; ++k) { sts128((qa ^ (k * 32)) + k * 256, qv[k]); m = umax4(qv[k], m); SB(); }
                    if (__all(m != POISON) || it >= SPIN_CAP) break;
                }
                STAMP(4);
            }
            BAR();
            const unsigned fo = W_PZF + (unsigned)(ow * 64 + l) * 16u;
            v4u f;
            if (ow >= 2) {
                vread();
            } else {
                if (ow == 0) gst64(rs, l == 0 ? W_TIME + wg * 128 : W_BYTES, (v2u){now16(), tag});
                __builtin_amdgcn_s_sleep(PZC_SLEEP);
                asm volatile("s_nop 4\n\tbuffer_load_dwordx4 %0, %1, %2, 0 offen sc1" : "=v"(f) : "v"(fo), "s"(mksrd(ws, W_BYTES)) : "memory");
                const v4u srd = mksrd(wuv, 4194304);
                const unsigned vo = (unsigned)g * (16u * 8192u) + (unsigned)((p16 ^ (g << 2) ^ ow) * 16), so = (unsigned)(128 * qd + 64 * ow) * 8192u + hh * 256;
#pragma unroll
                for (int k = 0; k < 4; ++k) dma_loop(srd, vo ^ ((k & 1) * 32), so + 4 * k * 8192, 8192, L_WUV + (2 * k + ow) * 1024, 8192, 4);
            }
            unsigned po = W_PART + (unsigned)(((16 * g + ow) * 128 + arow) * 256 + p16 * 16), pfa = L_P + l * 16;
            PIN(po); PIN(pfa);
            STAMP(5);
            BAR();
            if (ow < 2) {
                asm volatile("s_waitcnt vmcnt(16)" : "+v"(f) :: "memory");
                bool ok = f.x == tag && f.y == tag && f.z == tag && f.w == tag;
                if (__builtin_expect(!__all(ok), 0)) {
                    _Pragma("unroll 1") for (int it = 0; it < SPIN_CAP; ++it) {
                        __builtin_amdgcn_s_sleep(2);
                        CFENCE();
                        const v4u f2 = gld128(rs, ok ? W_BYTES : fo);
                        ok = ok || (f2.x == tag && f2.y == tag && f2.z == tag && f2.w == tag);
                        if (__all(ok)) break;
                    }
                }
            } else {
                if (ow == 2) {
                    const f32x4 mv = __builtin_bit_cast(f32x4, lds128(L_MX + (l & 31) * 16));
                    const f32x4 lv = __builtin_bit_cast(f32x4, lds128(L_LS + (l & 31) * 32)) + __builtin_bit_cast(f32x4, lds128(L_LS + (l & 31) * 32 + 16));
                    const float M = max2(max3(mv.x, mv.y, mv.z), mv.w);
                    const float L = (lv.x * __builtin_amdgcn_exp2f(mv.x - M) + lv.y * __builtin_amdgcn_exp2f(mv.y - M)) + (lv.z * __builtin_amdgcn_exp2f(mv.z - M) + lv.w * __builtin_amdgcn_exp2f(mv.w - M));
                    gst128(rs, l < 32 ? W_STAT + (unsigned)(arow * 512 + l * 16) : W_BYTES, (v4u){__float_as_uint(M), tag, __float_as_uint(L), tag});
                }
            }
            STAMP(14);
            __builtin_amdgcn_s_setprio(1);
            BAR();
            STAMP(6);
            v4u pf[8];
#pragma unroll
            for (int k = 0; k < 8; ++k) { pf[k] = lds128(pfa + ((k & 3) * 2 + (k >> 2)) * 1024); SB(); }
            f32x4 acc[2][8];
#pragma unroll
            for (int ct = 0; ct < 8; ++ct) { acc[0][ct] = (f32x4){0.f, 0.f, 0.f, 0.f}; acc[1][ct] = (f32x4){0.f, 0.f, 0.f, 0.f}; }
#pragma unroll
            for (int kg = 0; kg < 4; ++kg)
#pragma unroll
                for (int ct = 0; ct < 8; ++ct) acc[0][ct] = mfma16(pf[kg], vf[kg][ct], acc[0][ct]);
            SB();
            v4u oa[2][4];
#define PACK(h, j) oa[h][j] = (v4u){pk2(acc[h][0][j], acc[h][1][j]), pk2(acc[h][2][j], acc[h][3][j]), pk2(acc[h][4][j], acc[h][5][j]), pk2(acc[h][6][j], acc[h][7][j])}
#pragma unroll
            for (int kg = 0; kg < 4; ++kg) {
#pragma unroll
                for (int ct = 0; ct < 8; ++ct) {
                    acc[1][ct] = mfma16(pf[4 + kg], vf[kg][ct], acc[1][ct]);
                    if (kg < 2 && (ct & 3) == 1) { SB(); PACK(0, 2 * kg + (ct >> 2)); SB(); }
                    if (kg < 2 && (ct & 3) == 3) { SB(); gst128(rs, po, oa[0][2 * kg + (ct >> 2)], (2 * kg + (ct >> 2)) * 131072); SB(); }
                }
            }
            SB();
#pragma unroll
            for (int j = 0; j < 4; ++j) { PACK(1, j); gst128(rs, po, oa[1][j], 2097152 + j * 131072); SB(); }
            asm volatile("" :: "v"(oa[0][0]), "v"(oa[0][1]), "v"(oa[0][2]), "v"(oa[0][3]), "v"(oa[1][0]), "v"(oa[1][1]), "v"(oa[1][2]), "v"(oa[1][3]));
            __builtin_amdgcn_s_setprio(0);
            STAMP(8);
            VMWAIT(8);
        }
        {
            unsigned clr = L_CL + (l & 3) * 272 + g * 64;
            unsigned cto = p16 < 4 ? W_CTXP + (unsigned)((hh * 4 + qd) * 1024 + w * 128 + p16 * 32 + g * 8) : W_BYTES;
            PIN(clr); PIN(cto);
            v4u uf[4];
#define STOK(v) ((v).y == tag && (v).w == tag)
            if (w < 4) {
                const unsigned sto = W_STAT + (unsigned)((w * 32 + (l & 31)) * 512 + hh * 16);
                const unsigned pvo[1] = {W_PART + (unsigned)(((hh * 4 + qd) * 128 + w * 32) * 256 + l * 16)};
                BAR();
                STAMP(7);
                v4u st = gld128(rs, sto);
                SB();
#pragma unroll
                for (int s = 0; s < 4; ++s) uf[s] = ldstr2(L_WUV + tra + s * 8192, L_WUV + (tra ^ 16) + 1024 + s * 8192);
                const v4u sm = selmask(l, 0);
                unsigned cla = L_CL + w * 272 + p16 * 16 + (g & 1) * 8 + (g >> 1) * 4096;
                PIN(cla);
                v4u pv[8];
                if (!__all(STOK(st))) {
                    wait_until(hint() + D3);
                    STAMP(15);
                    CFENCE();
                    st = gld128(rs, sto);
                } else {
                    __builtin_amdgcn_s_sleep(PART_SLEEP);
                }
                SB();
                STAMP(13);
                gissue<8, 1>(rs, pvo, 1024, pv);
                SB();
                if (__builtin_expect(!__all(STOK(st)), 0)) {
                    _Pragma("unroll 1") for (int it = 0; it < SPIN_CAP; ++it) {
                        CFENCE();
                        if (!STOK(st)) st = gld128(rs, sto);
                        if (__all(STOK(st))) break;
                    }
                }
                STAMP(14);
                const float ms = __uint_as_float(st.x);
                const float Mx = xmax16(rmax16(ms));
                const unsigned cb2 = pk2(__builtin_amdgcn_exp2f(ms - Mx), __builtin_amdgcn_exp2f(ms - Mx));
                float rl = __builtin_amdgcn_rcpf(xsum16(rsum16(bflo(cb2) * __uint_as_float(st.z))));
                v4u cf[8];
#pragma unroll
                for (int t = 0; t < 8; ++t) { const unsigned c = (unsigned)__builtin_amdgcn_ds_bpermute((4 * t + g) * 4, (int)cb2); cf[t] = sm & c; PIN(cf[t]); }
                PIN(rl);
                SB();
                {
                    unsigned m = 0;
#pragma unroll
                    for (int k = 0; k < 8; ++k) m = umax4(pv[k], m);
                    STAMP(8);
                    if (__builtin_expect(!__all(m != POISON), 0)) {
                        _Pragma("unroll 1") for (int it = 0; it < SPIN_CAP; ++it) {
                            CFENCE();
#pragma unroll
                            for (int k = 0; k < 8; ++k) if (umax4(pv[k], 0u) == POISON) pv[k] = gld128(rs, pvo[0], k * 1024);
                            m = 0;
#pragma unroll
                            for (int k = 0; k < 8; ++k) m = umax4(pv[k], m);
                            if (__all(m != POISON)) break;
                        }
                    }
                    CFENCE();
                }
                STAMP(9);
                f32x4 acc = {0.f, 0.f, 0.f, 0.f};
#pragma unroll
                for (int t = 0; t < 8; ++t) acc = mfma16(cf[t], pv[t], acc);
                SB();
                sts64(cla, (v2u){pk2(acc.x * rl, acc.y * rl), pk2(acc.z * rl, acc.w * rl)});
            } else {
                BAR();
#pragma unroll
                for (int s = 0; s < 4; ++s) uf[s] = ldstr2(L_WUV + tra + s * 8192, L_WUV + (tra ^ 16) + 1024 + s * 8192);
            }
            BAR();
            v4u bf[4];
#pragma unroll
            for (int s = 0; s < 4; ++s) { bf[s] = lds128(clr + s * 16); SB(); }
            f32x4 c4 = {0.f, 0.f, 0.f, 0.f};
#pragma unroll
            for (int s = 0; s < 4; ++s) c4 = mfma16(uf[s], bf[s], c4);
            gst64(rs, cto, (v2u){pk2(c4.x, c4.y), pk2(c4.z, c4.w)});
        }
        STAMP(10);
        if (ix < 4 && w == 4) {
            const int r = xc * 4 + ix, oi = ((l >> 1) & 3) * 512 + r * 16 + (l & 1) * 8 + (g & 1) * 4;
            const v2u xr = *(const GAS v2u*)(x + oi);
            const v4u sel0 = selmask(l, 0) & 0x3F803F80u;
            wait_until(hint() + D6);
            STAMP(7);
            v4u v[8];
            { const unsigned o[1] = {W_OUTP + (unsigned)((l >> 3) * 4096 + r * 128 + (l & 7) * 16)}; gather<8, 1>(rs, o, 8 * 4096, v); }
            f32x4 acc = {0.f, 0.f, 0.f, 0.f};
#pragma unroll
            for (int t = 0; t < 8; ++t) acc = mfma16(sel0, v[t], acc);
            const float o0 = acc.x + dpp<0x128>(acc.x) + bflo(xr.x), o1 = acc.y + dpp<0x128>(acc.y) + bfhi(xr.x);
            const float o2 = acc.z + dpp<0x128>(acc.z) + bflo(xr.y), o3 = acc.w + dpp<0x128>(acc.w) + bfhi(xr.y);
            if (g < 2 && p16 < 8) *(GAS v2u*)(out + oi) = (v2u){pk2(o0, o1), pk2(o2, o3)};
            if (r == 0 && l == 0) gst32(rs, W_EPOCH, tag);
            STAMP(15);
        }
        STAMP_OUT();
        return;
    }

    const bool isO = ix < 8;
    const int fs = xc * 7 + (ix - 8);
    {
        constexpr unsigned L_WUK = 131072, L_QL = 122880, L_QA = L_QL + 6144;
        const v4u pz = {POISON, POISON, POISON, POISON};
        const unsigned t = threadIdx.x;
        auto pzfill = [&]() {
#pragma unroll
            for (int k = 0; k < 8; ++k) gst128(rs, W_PART + (unsigned)(wg - 128) * 32768u + (t - 256) * 16, pz, k * 4096);
        };
        if (w < 4) {
            gst128(rs, w == 0 ? W_CTXP + (unsigned)(hh * 4 + qd) * 1024u + l * 16 : W_BYTES, pz);
            gst128(rs, isO ? (t < 32 ? W_HP + (unsigned)(xc * 4096 + ix * 512) + t * 16 : W_BYTES) : (ix < 15 ? W_OUTP + (unsigned)fs * 4096u + t * 16 : W_BYTES), pz);
        }
        auto kvpf = [&](unsigned cb) {
            unsigned so = (unsigned)(arow * 128 + 32 * (w & 3)) * 1024u + cb * 256, n = 8;
            asm volatile("s_mov_b32 m0, %4\n\ts_nop 3\n"
                         ".Lpf%=:\n\t"
                         "buffer_load_dwordx4 %2, %3, %0 offen lds\n\t"
                         "s_add_u32 %0, %0, 0x1000\n\ts_sub_u32 %1, %1, 1\n\ts_cmp_lg_u32 %1, 0\n\ts_cbranch_scc1 .Lpf%="
                         : "+s"(so), "+s"(n) : "v"((unsigned)g * 1024u + p16 * 16), "s"(mksrd(ckv_arg, 16777216u)), "s"(110592 + w * 1024) : "scc", "memory");
        };
        if (w >= 4 && KVPF >= 1) { __builtin_amdgcn_s_sleep(KVPF_SLEEP); kvpf(3); }
        unsigned off = W_QP + (unsigned)(((hh * 4 + g) * 8 + 2 * w) * 256 + p16 * 16), qlw = L_QL + (g * 4 + (p16 >> 2)) * 272 + w * 64 + (p16 & 3) * 8, qlr = L_QL + p16 * 272 + g * 64;
        unsigned qaw = L_QA + (l & 3) * 256 + w * 32 + g * 8, qar = L_QA + l * 16;
        unsigned qo = W_QABS + (unsigned)((g * 32 + hh) * 1024 + qd * 256 + p16 * 16);
        PIN(off); PIN(qlw); PIN(qlr); PIN(qaw); PIN(qar); PIN(qo);
        GET_TAG();
        if (w < 4) VMWAIT(0);
        else if (PZ_AT == 0) { __builtin_amdgcn_s_sleep(PZ_SLEEP); pzfill(); }
        STAMP(1);
        BAR();
        v4u af[4];
        {
            const unsigned a = L_WUK + (16 * w + p16) * 256 + (((4 * g) ^ (((p16 & 3) << 2) | (p16 >> 2))) * 16);
#pragma unroll
            for (int s = 0; s < 4; ++s) af[s] = lds128(a ^ (s * 16));
        }
        STAMP(2);
        if (w < 4) {
            __builtin_amdgcn_s_sleep(HOP1_SLEEP);
            CFENCE();
            STAMP(3);
            v4u v = gld128(rs, off), u = gld128(rs, off + 256);
            if (__builtin_expect(!__all(v.y == tag && v.w == tag && u.y == tag && u.w == tag), 0)) {
                STAMP(4);
                _Pragma("unroll 1") for (int it = 0; it < SPIN_CAP; ++it) {
                    CFENCE();
                    if (v.y != tag || v.w != tag || u.y != tag || u.w != tag) { v = gld128(rs, off); u = gld128(rs, off + 256); }
                    if (__all(v.y == tag && v.w == tag && u.y == tag && u.w == tag)) break;
                }
            }
            STAMP(5);
            sts64(qlw, (v2u){v.x, v.z});
            sts64(qlw + 32, (v2u){u.x, u.z});
        }
        BAR();
        STAMP(14);
        {
            v4u bf[4];
#pragma unroll
            for (int s = 0; s < 4; ++s) { bf[s] = lds128(qlr + s * 16); SB(); }
            f32x4 acc = {0.f, 0.f, 0.f, 0.f};
#pragma unroll
            for (int s = 0; s < 4; ++s) acc = mfma16(af[s], bf[s], acc);
            acc *= 0.08838834764831845f * 1.4426950408889634f;
#pragma unroll
            for (int j = 0; j < 4; ++j) acc[j] += dpp<0x128>(acc[j]);
#pragma unroll
            for (int j = 0; j < 4; ++j) acc[j] += dpp<0x124>(acc[j]);
            sts64(qaw, (v2u){pk2(acc.x, acc.y), pk2(acc.z, acc.w)});
        }
        STAMP(13);
        BAR();
        if (w == 0) gst128(rs, qo, lds128(qar));
        STAMP(6);
        if (w < 4 && KVPF >= 2) kvpf(2);
        if (w >= 4) {
            if (PZ_AT == 1) { __builtin_amdgcn_s_sleep(PZ_SLEEP); pzfill(); }
            VMWAIT(0);
            if (l == 0) gst32(rs, W_PZF + (unsigned)(wg - 128) * 16u + (w - 4) * 4, tag);
            STAMP(12);
        }
    }
    if (ix == 15) {
        if (w == 0) {
            __builtin_amdgcn_s_sleep(AG_SLEEP);
            const unsigned o = W_TIME + l * 128;
            v2u a = gld64(rs, o), b = gld64(rs, o, 8192);
            _Pragma("unroll 1") for (int it = 0; it < SPIN_CAP; ++it) {
                if (__all(a.y == tag && b.y == tag)) break;
                CFENCE();
                if (a.y != tag) a = gld64(rs, o);
                if (b.y != tag) b = gld64(rs, o, 8192);
            }
            const unsigned n = now16();
            const float d = xmax32(xmax16(rmax16(fmaxf((float)(short)(a.x - n), (float)(short)(b.x - n)))));
            gst128(rs, l < 32 ? W_HINT + (unsigned)(l * 8 + xc) * 128u : W_BYTES, (v4u){(unsigned)((int)d + (int)n), tag, tag, tag});
            STAMP(8);
        }
        STAMP_OUT();
        return;
    }
    if (isO) __builtin_amdgcn_s_sleep(TAIL_SLEEP); else __builtin_amdgcn_s_sleep(FFN_SLEEP);
    constexpr unsigned L_W = 0, L_WD = 65536, L_ACT = 98304, L_GR = L_ACT + 8192, L_ST = L_GR + 2048;
    {
        const unsigned ch = (unsigned)((l & 7) ^ (((g & 1) | ((w & 1) << 1)) << 1));
        if (isO) {
            dma_paced(mksrd(wo, 4194304), (unsigned)(l >> 3) * 1024u + ch * 16, (unsigned)(512 * xc + 8 * w) * 1024u + ix * 128, 65536, L_W + w * 1024, 8192, 8);
        } else {
            const unsigned char* src = (const unsigned char*)(ch < 4 ? wg_ : wu_) + (size_t)(8 * w + (l >> 3)) * 3584 + fs * 64 + (ch & 3) * 16;
            _Pragma("unroll 1") for (int t = 0; t < 8; ++t) { dmag(src, L_W + (8 * t + w) * 1024); src += 64 * 3584; VMWAIT(FFN_DEPTH); }
            const v4u srd = mksrd(wd, 1835008);
            const unsigned sw = (unsigned)(((2 * w) & 3) << 2 | (w >> 1));
            dma_paced(srd, (l ^ sw) * 16, (unsigned)(32 * fs + 2 * w) * 1024u, 16384, L_WD + 2 * w * 1024, 16384, 2);
            dma_paced(srd, (l ^ sw ^ 4) * 16, (unsigned)(32 * fs + 2 * w + 1) * 1024u, 16384, L_WD + (2 * w + 1) * 1024, 16384, 2);
        }
        VMWAIT(0);
    }
    STAMP(7);
    BAR();
    v4u wf[16];
    {
        const unsigned a0 = L_W + (8 * g + trq) * 128 + (((isO ? 2 * (w & 3) + (trp >> 1) : (w & 3) + 4 * (trp >> 1)) ^ ((((trq >> 1) & 1) | ((g & 1) << 1)) << 1)) * 16) + 8 * (trp & 1);
#pragma unroll
        for (int s = 0; s < 16; ++s) wf[s] = ldstr2(a0 + s * 4096, a0 + s * 4096 + 512);
    }
    const v4u one = {0x3F803F80u, 0x3F803F80u, 0x3F803F80u, 0x3F803F80u};
    const v4u sel0 = selmask(l, 0) & one, sel1 = selmask(l, 8) & one;
    const int pb = (p16 >> 1) & 3, pk = 32 * (g >> 1) + 16 * (p16 >> 3) + 8 * (p16 & 1) + 4 * (g & 1);
    unsigned acta = L_ACT + pb * 1088 + (w * 64 + pk) * 2;
    PIN(acta);
    if (isO) {
        unsigned bfa = L_ACT + (l & 3) * 1088 + g * 16, ho = p16 < 4 ? W_HP + (unsigned)(xc * 4096 + (ix * 4 + w) * 128 + p16 * 32 + g * 8) : W_BYTES;
        PIN(bfa); PIN(ho);
        if (w == 7) wait_until(hint() + D4);
        BAR();
        STAMP(8);
        v4u v[2];
        { const unsigned o[1] = {W_CTXP + (unsigned)(((4 * xc + (w >> 1)) * 4 + g) * 1024 + (w & 1) * 512 + p16 * 16)}; gather<2, 1>(rs, o, 256, v); }
        STAMP(9);
        f32x4 acc = {0.f, 0.f, 0.f, 0.f};
        acc = mfma16(sel0, v[0], acc); acc = mfma16(sel1, v[1], acc);
        sts64(acta, (v2u){pk2(acc.x, acc.y), pk2(acc.z, acc.w)});
        BAR();
        if (w < 4) {
            v4u bf[16];
#pragma unroll
            for (int s = 0; s < 8; ++s) { bf[s] = lds128(bfa + s * 64); SB(); }
            f32x4 d = {0.f, 0.f, 0.f, 0.f};
#pragma unroll
            for (int s = 0; s < 16; ++s) {
                d = mfma16(wf[s], bf[s], d);
                SB();
                if (s < 8) bf[s + 8] = lds128(bfa + (s + 8) * 64);
                SB();
            }
            gst64(rs, ho, (v2u){pk2(d.x, d.y), pk2(d.z, d.w)});
        }
        STAMP(10);
    } else {
        v4u df[4];
        {
            const unsigned r0 = 8 * g + trq;
#pragma unroll
            for (int k = 0; k < 4; ++k) {
                const unsigned ch = 8 * w + 4 * (k >> 1) + trp;
                const unsigned a0 = L_WD + r0 * 1024 + ((ch ^ ((trq << 2) | ((2 * g) & 3))) * 16) + 8 * (k & 1);
                df[k] = ldstr2(a0, (a0 ^ 16) + 4096);
            }
        }
        const v2u xr = *(const GAS v2u*)(x + pb * 512 + w * 64 + pk);
        f32x4 cs = {0.f, 0.f, 0.f, 0.f};
#pragma unroll
        for (int s = 0; s < 16; ++s) cs = mfma16(wf[s], one, cs);
        unsigned sta = L_ST + pb * 272 + (w * 4 + g) * 8, str = L_ST + (l & 3) * 272 + (l >> 2) * 16, bfa = L_ACT + (l & 3) * 1088 + g * 16;
        unsigned a2w = L_GR + (l & 3) * 80 + w * 16 + (g & 1) * 8 + (g >> 1) * 4, a2r = L_GR + (l & 3) * 80 + g * 16;
        unsigned oo = p16 < 4 ? W_OUTP + (unsigned)(fs * 4096 + w * 512 + (g >> 1) * 128 + p16 * 32 + (g & 1) * 16) : W_BYTES;
        f32x4 acc = {bflo(xr.x), bfhi(xr.x), bflo(xr.y), bfhi(xr.y)};
        PIN(cs); PIN(sta); PIN(str); PIN(bfa); PIN(a2w); PIN(a2r); PIN(oo); PIN(acc);
        if (w == 7) wait_until(hint() + D5);
        BAR();
        STAMP(8);
        v4u v[4];
        {
            const unsigned off = W_HP + (unsigned)(g * 4096 + w * 512 + p16 * 16);
            const unsigned o[2] = {off, off + 16384};
            gather<4, 2>(rs, o, 256, v);
        }
        STAMP(9);
        acc = mfma16(sel0, v[0], acc); acc = mfma16(sel0, v[1], acc); acc = mfma16(sel1, v[2], acc); acc = mfma16(sel1, v[3], acc);
        sts64(acta, (v2u){pk2(acc.x, acc.y), pk2(acc.z, acc.w)});
        {
            float s1 = (acc.x + acc.y) + (acc.z + acc.w), s2 = acc.x * acc.x + acc.y * acc.y + acc.z * acc.z + acc.w * acc.w;
            s1 += dpp<0xB1>(s1); s2 += dpp<0xB1>(s2); s1 += dpp<0x128>(s1); s2 += dpp<0x128>(s2);
            sts64(sta, (v2u){__float_as_uint(s1), __float_as_uint(s2)});
        }
        BAR();
        if (w < 4) {
            v4u bf[16];
            bf[0] = lds128(bfa); SB();
            const f32x4 sv = __builtin_bit_cast(f32x4, lds128(str)); SB();
#pragma unroll
            for (int s = 1; s < 8; ++s) { bf[s] = lds128(bfa + s * 64); SB(); }
            f32x4 d = {0.f, 0.f, 0.f, 0.f};
            float s1, s2, mean, r2;
#pragma unroll
            for (int s = 0; s < 16; ++s) {
                d = mfma16(wf[s], bf[s], d);
                SB();
                if (s < 8) bf[s + 8] = lds128(bfa + (s + 8) * 64);
                if (s == 1) { s1 = sv.x + sv.z; s2 = sv.y + sv.w; }
                if (s == 2) { s1 += dpp<0x128>(s1); s2 += dpp<0x128>(s2); }
                if (s == 3) { s1 += dpp<0x124>(s1); s2 += dpp<0x124>(s2); }
                if (s == 5) s1 = xsum16(s1);
                if (s == 6) s2 = xsum16(s2);
                if (s == 8) s1 = xsum32(s1);
                if (s == 9) s2 = xsum32(s2);
                if (s == 10) { mean = s1 * (1.f / 512.f); s2 = s2 * (1.f / 512.f) - mean * mean; }
                if (s == 11) { r2 = __builtin_amdgcn_rcpf(max2(s2, 0.f) + 1e-5f); PIN(r2); }
                if (s == 12) { cs *= mean; PIN(cs); }
                SB();
            }
            const f32x4 y = d - cs;
            auto r0 = __builtin_amdgcn_permlane32_swap(__float_as_uint(y.x), __float_as_uint(y.z), false, false);
            auto r1 = __builtin_amdgcn_permlane32_swap(__float_as_uint(y.y), __float_as_uint(y.w), false, false);
            sts32(a2w, __uint_as_float(pk2(max2(__uint_as_float(r0[0]), 0.f) * (__uint_as_float(r0[1]) * r2), max2(__uint_as_float(r1[0]), 0.f) * (__uint_as_float(r1[1]) * r2))));
        }
        BAR();
        STAMP(10);
        {
            const v4u af = lds128(a2r);
            const f32x4 z = {0.f, 0.f, 0.f, 0.f};
            const f32x4 d0 = mfma16(df[0], af, z), d1 = mfma16(df[1], af, z), d2 = mfma16(df[2], af, z), d3 = mfma16(df[3], af, z);
            SB();
            const v4u o0 = {pk2(d0.x, d0.y), pk2(d0.z, d0.w), pk2(d1.x, d1.y), pk2(d1.z, d1.w)}, o1 = {pk2(d2.x, d2.y), pk2(d2.z, d2.w), pk2(d3.x, d3.y), pk2(d3.z, d3.w)};
            gst128(rs, oo, o0);
            gst128(rs, oo + 256, o1);
        }
        STAMP(11);
    }
    STAMP_OUT();
}

extern "C" void kernel_launch(void* const* d_in, const int* in_sizes, int n_in, void* d_out, int out_size, void* d_ws, size_t ws_size,
                              hipStream_t stream) {
    (void)in_sizes; (void)n_in; (void)out_size;
    unsigned char* arena = (unsigned char*)d_ws + ((ws_size - W_BYTES) & ~(size_t)4095);
    (void)hipFuncSetAttribute((const void*)fused_decode, hipFuncAttributeMaxDynamicSharedMemorySize, LDS_BYTES);
    fused_decode<<<256, 512, LDS_BYTES, stream>>>((const u16*)d_in[2], (const u16*)d_in[3], (const u16*)d_in[1], (const u16*)d_in[0], arena, (const u16*)d_in[4],
                                                  (const u16*)d_in[5], (const u16*)d_in[7], (const u16*)d_in[8], (const u16*)d_in[9], (u16*)d_out);
}
```
